```python
import torch

def _softmax(x, axis=-1):
    return torch.softmax(x, dim=axis)
def _gelu(x):
    return torch.nn.functional.gelu(x, approximate="tanh")
def _silu(x):
    return torch.nn.functional.silu(x)
def _sigmoid(x):
    return torch.sigmoid(x)
def _layernorm(x, axis=-1, eps=1e-5):
    m = x.mean(dim=axis, keepdim=True)
    v = x.var(dim=axis, keepdim=True, unbiased=False)
    return (x - m) / torch.sqrt(v + eps)
def _scatter_add(tgt, idx, src, axis=0):
    return tgt.clone().index_add_(axis, idx, src)
def _segment_sum(src, seg, n):
    out = torch.zeros((n,) + src.shape[1:], dtype=src.dtype)
    return out.index_add_(0, seg, src)
def _pad(x, pw):
    flat = tuple(v for pair in reversed(pw) for v in pair)
    return torch.nn.functional.pad(x, flat)

kernel_name = 'gated_mix__grouped_kv_mqa__glu_gated_swiglu__prompt'

def setup_inputs():

    _w = (lambda a: (lambda t: t.to(torch.bfloat16) if t.is_floating_point() else t)(torch.as_tensor(a)))
    def _A_setup():
        B, S, D, H = 1, 2048, 640, 32
        Dh = D // H
        return {
            "x": _w((lambda *s: torch.randn(*s, dtype=torch.bfloat16) * 0.05)(B, S, D)),
            "wq": _w((lambda *s: torch.randn(*s, dtype=torch.bfloat16) * 0.8)(D, H * Dh)),
            "wk": _w((lambda *s: torch.randn(*s, dtype=torch.bfloat16) * 0.8)(D, Dh)),
            "wv": _w((lambda *s: torch.randn(*s, dtype=torch.bfloat16) * 0.05)(D, Dh)),
            "wo": _w((lambda *s: torch.randn(*s, dtype=torch.bfloat16) * 0.05)(H * Dh, D)),
        }
    _A_in = _A_setup()

    def _B_setup():
        B, S, D, F = 1, 2048, 640, 1536
        return {
            "x": _w((lambda *s: torch.randn(*s, dtype=torch.bfloat16) * 0.05)(B, S, D)),
            "w1": _w((lambda *s: torch.randn(*s, dtype=torch.bfloat16) * 0.8)(D, F) * (2.0 / D) ** 0.5),
            "w3": _w((lambda *s: torch.randn(*s, dtype=torch.bfloat16) * 0.8)(D, F) * (2.0 / D) ** 0.5),
            "w2": _w((lambda *s: torch.randn(*s, dtype=torch.bfloat16) * 0.8)(F, D) * (2.0 / F) ** 0.5),
        }
    _B_in = _B_setup()

    return {
        "A_x": _A_in["x"],
        "A_wq": _A_in["wq"],
        "A_wk": _A_in["wk"],
        "A_wv": _A_in["wv"],
        "A_wo": _A_in["wo"],
        "B_x": _B_in["x"],
        "B_w1": _B_in["w1"],
        "B_w3": _B_in["w3"],
        "B_w2": _B_in["w2"],
        "gate_w": _w((lambda *s: torch.randn(*s, dtype=torch.bfloat16) * 0.05)(640) * 16.0),
    }

def reference(A_x, A_wq, A_wk, A_wv, A_wo, B_x, B_w1, B_w3, B_w2, gate_w):
    def _A_ref(x, wq, wk, wv, wo):
        B, S, D = x.shape
        H = 32
        Dh = D // H
        q = (lambda x, *d: x.permute(*d))((x @ wq).reshape(B, S, H, Dh), 0, 2, 1, 3)
        k = x @ wk
        v = x @ wv
        scores = torch.einsum("bhqd,bkd->bhqk", q, k) * (Dh ** -0.5)
        p = _softmax(scores, axis=-1)
        o = torch.einsum("bhqk,bkd->bhqd", p, v)
        return (lambda x, *d: x.permute(*d))(o, 0, 2, 1, 3).reshape(B, S, H * Dh) @ wo

    def _B_ref(x, w1, w3, w2):
        gate = torch.einsum("bsd,df->bsf", x, w1)
        up = torch.einsum("bsd,df->bsf", x, w3)
        h = _silu(gate) * up
        out = torch.einsum("bsf,fd->bsd", h, w2)
        return out

    x = A_x
    g = _sigmoid((x * gate_w).sum(-1, keepdims=True))
    a = _A_ref(x, A_wq, A_wk, A_wv, A_wo)
    b = _B_ref(x, B_w1, B_w3, B_w2)
    return g * a + (1 - g) * b
```

```cpp
#include <hip/hip_runtime.h>
#include <hip/hip_bf16.h>
typedef unsigned short u16;
__device__ __forceinline__ float b2f(u16 x){ return __builtin_bit_cast(float, (unsigned)x << 16); }
__device__ __forceinline__ u16 f2b(float f){ __bf16 b = (__bf16)f; return __builtin_bit_cast(u16, b); }
__global__ void gemm(const u16* A, const u16* B, float* C, int M, int N, int K, int ldc) {
  int n = blockIdx.x * 64 + (threadIdx.x & 63), m = blockIdx.y * 4 + (threadIdx.x >> 6);
  if (n >= N) return;
  float s = 0; for (int k = 0; k < K; k++) s += b2f(A[m*K+k]) * b2f(B[k*N+n]);
  C[m*ldc+n] = s;
}
__global__ void attn(const float* q, const float* k, const float* v, u16* o) {
  int r = blockIdx.x * 256 + threadIdx.x; int t = r >> 5, h = r & 31;
  float qq[20], acc[20]; for (int d=0; d<20; d++) { qq[d] = q[t*640+h*20+d] * 0.22360679775f; acc[d]=0; }
  float m = -1e30f, l = 0;
  for (int j=0;j<2048;j++){ float s=0; for (int d=0;d<20;d++) s += qq[d]*k[j*20+d];
    float mn = fmaxf(m,s); float a = __expf(m-mn), p = __expf(s-mn); l = l*a+p; for (int d=0;d<20;d++) acc[d] = acc[d]*a + p*v[j*20+d]; m = mn; }
  for (int d=0;d<20;d++) o[t*640+h*20+d] = f2b(acc[d]/l);
}
__global__ void act(const float* g, const float* u, u16* h) { int i = blockIdx.x*256+threadIdx.x; float x=g[i]; h[i] = f2b(x/(1.f+__expf(-x))*u[i]); }
__global__ void fin(const u16* x, const u16* gw, const float* a, const float* b, u16* out) {
  int t = blockIdx.x; __shared__ float s[64]; float p=0; for (int d=threadIdx.x; d<640; d+=64) p += b2f(x[t*640+d])*b2f(gw[d]);
  for (int o=32;o;o>>=1) p += __shfl_xor(p,o);
  float g = 1.f/(1.f+__expf(-p));
  for (int d=threadIdx.x; d<640; d+=64) out[t*640+d] = f2b(g*a[t*640+d] + (1-g)*b[t*640+d]);
}
extern "C" void kernel_launch(void* const* d_in, const int* in_sizes, int n_in, void* d_out, int out_size, void* d_ws, size_t ws_size, hipStream_t stream) {
  const u16 *x=(const u16*)d_in[0], *wq=(const u16*)d_in[1], *wk=(const u16*)d_in[2], *wv=(const u16*)d_in[3], *wo=(const u16*)d_in[4], *w1=(const u16*)d_in[6], *w3=(const u16*)d_in[7], *w2=(const u16*)d_in[8], *gw=(const u16*)d_in[9];
  float* ws=(float*)d_ws; float *q=ws, *k=q+2048*640, *v=k+2048*20, *g=v+2048*20, *u=g+2048*1536, *a=u+2048*1536, *b=a+2048*640; u16* o=(u16*)(b+2048*640); u16* h=o+2048*640;
  gemm<<<dim3(10,512),256,0,stream>>>(x,wq,q,2048,640,640,640);
  gemm<<<dim3(1,512),256,0,stream>>>(x,wk,k,2048,20,640,20);
  gemm<<<dim3(1,512),256,0,stream>>>(x,wv,v,2048,20,640,20);
  gemm<<<dim3(24,512),256,0,stream>>>(x,w1,g,2048,1536,640,1536);
  gemm<<<dim3(24,512),256,0,stream>>>(x,w3,u,2048,1536,640,1536);
  attn<<<256,256,0,stream>>>(q,k,v,o);
  act<<<2048*6,256,0,stream>>>(g,u,h);
  gemm<<<dim3(10,512),256,0,stream>>>(o,wo,a,2048,640,640,640);
  gemm<<<dim3(10,512),256,0,stream>>>(h,w2,b,2048,640,1536,640);
  fin<<<2048,64,0,stream>>>(x,gw,a,b,(u16*)d_out);
}
```

```cpp
#include <hip/hip_runtime.h>
#include <hip/hip_bf16.h>

typedef unsigned u32; typedef unsigned long long u64;
typedef __bf16 bf16x8 __attribute__((ext_vector_type(8)));
typedef __bf16 bf16x2 __attribute__((ext_vector_type(2)));
typedef short s16x4 __attribute__((ext_vector_type(4)));
typedef float f32x2 __attribute__((ext_vector_type(2)));
typedef float f32x4 __attribute__((ext_vector_type(4)));
typedef float f32x16 __attribute__((ext_vector_type(16)));
typedef u32 u32x2 __attribute__((ext_vector_type(2)));
typedef u32 u32x4 __attribute__((ext_vector_type(4)));
typedef u32 u32x8 __attribute__((ext_vector_type(8)));
typedef float f32x12 __attribute__((ext_vector_type(12)));
typedef u32 u32x16 __attribute__((ext_vector_type(16)));
typedef __amdgpu_buffer_rsrc_t rsrc_t;
#define LAS __attribute__((address_space(3)))
#define GAS __attribute__((address_space(1)))
#define DI __device__ __forceinline__
#define SB() __builtin_amdgcn_sched_barrier(0)
#define RLX __ATOMIC_RELAXED, __HIP_MEMORY_SCOPE_AGENT

#define P1_ASM \
  "s_mov_b32 s46, %[st]\n\t" \
  "s_lshl_b32 s40, s46, 7\n\t" \
  "s_mul_i32 s41, s46, 196608\n\t" \
  "s_mul_i32 s42, s46, 81920\n\t" \
  "s_mov_b32 s50, 0\n\t" \
  "s_mov_b32 s51, 0\n\t" \
  "s_cmp_lt_u32 %[wv], 5\n\t" \
  "s_cselect_b64 s[52:53], -1, 0\n\t" \
  "s_cmp_lt_u32 %[wv], 6\n\t" \
  "s_cbranch_scc0 LP_q_in_%=\n\t" \
  "LP_u_in_%=:\n\t" \
  "ds_read_b128 v[64:67], v96 offset:0\n\t" \
  "ds_read_b128 v[68:71], v96 offset:4096\n\t" \
  "ds_read_b64_tr_b16 v[72:73], v104 offset:0\n\t" \
  "ds_read_b64_tr_b16 v[74:75], v104 offset:768\n\t" \
  "ds_read_b64_tr_b16 v[76:77], v105 offset:0\n\t" \
  "ds_read_b64_tr_b16 v[78:79], v105 offset:768\n\t" \
  "s_mov_b64 vcc, s[52:53]\n\t" \
  "s_add_u32 m0, %[lx], 92160\n\t" \
  "s_nop 0\n\t" \
  "buffer_load_dwordx4 v108, %[rx], s40 offen offset:0 lds\n\t" \
  "buffer_load_dwordx4 v109, %[rx], s40 offen offset:1024 lds\n\t" \
  "s_add_u32 m0, %[lw], 92160\n\t" \
  "s_waitcnt lgkmcnt(0)\n\t" \
  "v_mfma_f32_32x32x16_bf16 v[0:15], v[72:75], v[64:67], 0\n\t" \
  "ds_read_b128 v[80:83], v97 offset:0\n\t" \
  "ds_read_b128 v[84:87], v97 offset:4096\n\t" \
  "v_mfma_f32_32x32x16_bf16 v[16:31], v[76:79], v[64:67], 0\n\t" \
  "ds_read_b64_tr_b16 v[88:89], v104 offset:3072\n\t" \
  "ds_read_b64_tr_b16 v[90:91], v104 offset:3840\n\t" \
  "buffer_load_dwordx4 v110, %[rw], s41 offen offset:0 lds\n\t" \
  "v_mfma_f32_32x32x16_bf16 v[32:47], v[72:75], v[68:71], 0\n\t" \
  "ds_read_b64_tr_b16 v[92:93], v105 offset:3072\n\t" \
  "ds_read_b64_tr_b16 v[94:95], v105 offset:3840\n\t" \
  "v_mfma_f32_32x32x16_bf16 v[48:63], v[76:79], v[68:71], 0\n\t" \
  "buffer_load_dwordx4 v111, %[rw], s41 offen offset:1024 lds\n\t" \
  "s_waitcnt lgkmcnt(0)\n\t" \
  "v_mfma_f32_32x32x16_bf16 v[0:15], v[88:91], v[80:83], v[0:15]\n\t" \
  "ds_read_b128 v[64:67], v98 offset:0\n\t" \
  "ds_read_b128 v[68:71], v98 offset:4096\n\t" \
  "v_mfma_f32_32x32x16_bf16 v[16:31], v[92:95], v[80:83], v[16:31]\n\t" \
  "ds_read_b64_tr_b16 v[72:73], v104 offset:6144\n\t" \
  "ds_read_b64_tr_b16 v[74:75], v104 offset:6912\n\t" \
  "buffer_load_dwordx4 v112, %[rw], s41 offen offset:2048 lds\n\t" \
  "s_add_u32 m0, %[lq], 92160\n\t" \
  "v_mfma_f32_32x32x16_bf16 v[32:47], v[88:91], v[84:87], v[32:47]\n\t" \
  "ds_read_b64_tr_b16 v[76:77], v105 offset:6144\n\t" \
  "ds_read_b64_tr_b16 v[78:79], v105 offset:6912\n\t" \
  "v_mfma_f32_32x32x16_bf16 v[48:63], v[92:95], v[84:87], v[48:63]\n\t" \
  "s_cbranch_vccz LP_d1_%=\n\t" \
  "buffer_load_dwordx4 v113, %[rq], s42 offen lds\n\t" \
  "LP_d1_%=:\n\t" \
  "s_add_u32 s50, s50, 1\n\t" \
  "s_cmp_lt_u32 s50, 8\n\t" \
  "s_cselect_b64 vcc, -1, 0\n\t" \
  "LP_u_loop_%=:\n\t" \
  "s_waitcnt lgkmcnt(0)\n\t" \
  "v_mfma_f32_32x32x16_bf16 v[0:15], v[72:75], v[64:67], v[0:15]\n\t" \
  "ds_read_b128 v[80:83], v99 offset:0\n\t" \
  "ds_read_b128 v[84:87], v99 offset:4096\n\t" \
  "ds_read_b64_tr_b16 v[88:89], v104 offset:9216\n\t" \
  "ds_read_b64_tr_b16 v[90:91], v104 offset:9984\n\t" \
  "v_mfma_f32_32x32x16_bf16 v[16:31], v[76:79], v[64:67], v[16:31]\n\t" \
  "ds_read_b64_tr_b16 v[92:93], v105 offset:9216\n\t" \
  "ds_read_b64_tr_b16 v[94:95], v105 offset:9984\n\t" \
  "s_add_u32 s46, s46, 1\n\t" \
  "s_cmp_eq_u32 s46, 10\n\t" \
  "s_cselect_b32 s46, 0, s46\n\t" \
  "v_mfma_f32_32x32x16_bf16 v[32:47], v[72:75], v[68:71], v[32:47]\n\t" \
  "s_lshl_b32 s40, s46, 7\n\t" \
  "s_mul_i32 s41, s46, 196608\n\t" \
  "s_mul_i32 s42, s46, 81920\n\t" \
  "s_add_u32 m0, %[lx], 0\n\t" \
  "v_mfma_f32_32x32x16_bf16 v[48:63], v[76:79], v[68:71], v[48:63]\n\t" \
  "s_waitcnt vmcnt(5) lgkmcnt(0)\n\t" \
  "s_barrier\n\t" \
  "v_mfma_f32_32x32x16_bf16 v[0:15], v[88:91], v[80:83], v[0:15]\n\t" \
  "ds_read_b128 v[64:67], v96 offset:46080\n\t" \
  "ds_read_b128 v[68:71], v96 offset:50176\n\t" \
  "v_mfma_f32_32x32x16_bf16 v[16:31], v[92:95], v[80:83], v[16:31]\n\t" \
  "ds_read_b64_tr_b16 v[72:73], v104 offset:46080\n\t" \
  "ds_read_b64_tr_b16 v[74:75], v104 offset:46848\n\t" \
  "s_cmp_eq_u32 s50, %[pub]\n\t" \
  "s_cbranch_scc1 LP_pub_%=\n\t" \
  "LP_pubd_%=:\n\t" \
  "buffer_load_dwordx4 v108, %[rx], s40 offen offset:0 lds\n\t" \
  "v_mfma_f32_32x32x16_bf16 v[32:47], v[88:91], v[84:87], v[32:47]\n\t" \
  "ds_read_b64_tr_b16 v[76:77], v105 offset:46080\n\t" \
  "ds_read_b64_tr_b16 v[78:79], v105 offset:46848\n\t" \
  "v_mfma_f32_32x32x16_bf16 v[48:63], v[92:95], v[84:87], v[48:63]\n\t" \
  "buffer_load_dwordx4 v109, %[rx], s40 offen offset:1024 lds\n\t" \
  "s_add_u32 m0, %[lw], 0\n\t" \
  "s_waitcnt lgkmcnt(0)\n\t" \
  "v_mfma_f32_32x32x16_bf16 v[0:15], v[72:75], v[64:67], v[0:15]\n\t" \
  "ds_read_b128 v[80:83], v97 offset:46080\n\t" \
  "ds_read_b128 v[84:87], v97 offset:50176\n\t" \
  "v_mfma_f32_32x32x16_bf16 v[16:31], v[76:79], v[64:67], v[16:31]\n\t" \
  "ds_read_b64_tr_b16 v[88:89], v104 offset:49152\n\t" \
  "ds_read_b64_tr_b16 v[90:91], v104 offset:49920\n\t" \
  "buffer_load_dwordx4 v110, %[rw], s41 offen offset:0 lds\n\t" \
  "v_mfma_f32_32x32x16_bf16 v[32:47], v[72:75], v[68:71], v[32:47]\n\t" \
  "ds_read_b64_tr_b16 v[92:93], v105 offset:49152\n\t" \
  "ds_read_b64_tr_b16 v[94:95], v105 offset:49920\n\t" \
  "v_mfma_f32_32x32x16_bf16 v[48:63], v[76:79], v[68:71], v[48:63]\n\t" \
  "buffer_load_dwordx4 v111, %[rw], s41 offen offset:1024 lds\n\t" \
  "s_waitcnt lgkmcnt(0)\n\t" \
  "v_mfma_f32_32x32x16_bf16 v[0:15], v[88:91], v[80:83], v[0:15]\n\t" \
  "ds_read_b128 v[64:67], v98 offset:46080\n\t" \
  "ds_read_b128 v[68:71], v98 offset:50176\n\t" \
  "v_mfma_f32_32x32x16_bf16 v[16:31], v[92:95], v[80:83], v[16:31]\n\t" \
  "ds_read_b64_tr_b16 v[72:73], v104 offset:52224\n\t" \
  "ds_read_b64_tr_b16 v[74:75], v104 offset:52992\n\t" \
  "buffer_load_dwordx4 v112, %[rw], s41 offen offset:2048 lds\n\t" \
  "s_add_u32 m0, %[lq], 0\n\t" \
  "s_and_b64 vcc, vcc, s[52:53]\n\t" \
  "v_mfma_f32_32x32x16_bf16 v[32:47], v[88:91], v[84:87], v[32:47]\n\t" \
  "ds_read_b64_tr_b16 v[76:77], v105 offset:52224\n\t" \
  "ds_read_b64_tr_b16 v[78:79], v105 offset:52992\n\t" \
  "v_mfma_f32_32x32x16_bf16 v[48:63], v[92:95], v[84:87], v[48:63]\n\t" \
  "s_cbranch_vccz LP_d2_%=\n\t" \
  "buffer_load_dwordx4 v113, %[rq], s42 offen lds\n\t" \
  "LP_d2_%=:\n\t" \
  "s_add_u32 s50, s50, 1\n\t" \
  "s_cmp_lt_u32 s50, 8\n\t" \
  "s_cselect_b64 vcc, -1, 0\n\t" \
  "s_waitcnt lgkmcnt(0)\n\t" \
  "v_mfma_f32_32x32x16_bf16 v[0:15], v[72:75], v[64:67], v[0:15]\n\t" \
  "ds_read_b128 v[80:83], v99 offset:46080\n\t" \
  "ds_read_b128 v[84:87], v99 offset:50176\n\t" \
  "ds_read_b64_tr_b16 v[88:89], v104 offset:55296\n\t" \
  "ds_read_b64_tr_b16 v[90:91], v104 offset:56064\n\t" \
  "v_mfma_f32_32x32x16_bf16 v[16:31], v[76:79], v[64:67], v[16:31]\n\t" \
  "ds_read_b64_tr_b16 v[92:93], v105 offset:55296\n\t" \
  "ds_read_b64_tr_b16 v[94:95], v105 offset:56064\n\t" \
  "s_add_u32 s46, s46, 1\n\t" \
  "s_cmp_eq_u32 s46, 10\n\t" \
  "s_cselect_b32 s46, 0, s46\n\t" \
  "v_mfma_f32_32x32x16_bf16 v[32:47], v[72:75], v[68:71], v[32:47]\n\t" \
  "s_lshl_b32 s40, s46, 7\n\t" \
  "s_mul_i32 s41, s46, 196608\n\t" \
  "s_mul_i32 s42, s46, 81920\n\t" \
  "s_add_u32 m0, %[lx], 46080\n\t" \
  "v_mfma_f32_32x32x16_bf16 v[48:63], v[76:79], v[68:71], v[48:63]\n\t" \
  "s_waitcnt vmcnt(5) lgkmcnt(0)\n\t" \
  "s_barrier\n\t" \
  "v_mfma_f32_32x32x16_bf16 v[0:15], v[88:91], v[80:83], v[0:15]\n\t" \
  "ds_read_b128 v[64:67], v100 offset:0\n\t" \
  "ds_read_b128 v[68:71], v100 offset:4096\n\t" \
  "v_mfma_f32_32x32x16_bf16 v[16:31], v[92:95], v[80:83], v[16:31]\n\t" \
  "ds_read_b64_tr_b16 v[72:73], v106 offset:0\n\t" \
  "ds_read_b64_tr_b16 v[74:75], v106 offset:768\n\t" \
  "s_cmp_eq_u32 s50, %[dp]\n\t" \
  "s_cbranch_scc0 LP_p0_%=\n\t" \
  "s_mov_b32 m0, %[lk]\n\t" \
  "s_nop 0\n\t" \
  "global_load_lds_dwordx4 v127, %[gk] offset:0\n\t" \
  "global_load_lds_dwordx4 v128, %[gk] offset:1024\n\t" \
  "global_load_lds_dwordx4 v129, %[gk] offset:2048\n\t" \
  "global_load_lds_dwordx4 v130, %[gk] offset:3072\n\t" \
  "s_add_u32 m0, m0, 4096\n\t" \
  "s_nop 0\n\t" \
  "global_load_lds_dwordx4 v131, %[gk]\n\t" \
  "LP_p0_%=:\n\t" \
  "s_cbranch_vccz LP_d3_%=\n\t" \
  "buffer_load_dwordx4 v108, %[rx], s40 offen offset:0 lds\n\t" \
  "LP_d3_%=:\n\t" \
  "v_mfma_f32_32x32x16_bf16 v[32:47], v[88:91], v[84:87], v[32:47]\n\t" \
  "ds_read_b64_tr_b16 v[76:77], v107 offset:0\n\t" \
  "ds_read_b64_tr_b16 v[78:79], v107 offset:768\n\t" \
  "v_mfma_f32_32x32x16_bf16 v[48:63], v[92:95], v[84:87], v[48:63]\n\t" \
  "s_cbranch_vccz LP_d4_%=\n\t" \
  "buffer_load_dwordx4 v109, %[rx], s40 offen offset:1024 lds\n\t" \
  "LP_d4_%=:\n\t" \
  "s_add_u32 m0, %[lw], 46080\n\t" \
  "s_waitcnt lgkmcnt(0)\n\t" \
  "v_mfma_f32_32x32x16_bf16 v[0:15], v[72:75], v[64:67], v[0:15]\n\t" \
  "ds_read_b128 v[80:83], v101 offset:0\n\t" \
  "ds_read_b128 v[84:87], v101 offset:4096\n\t" \
  "v_mfma_f32_32x32x16_bf16 v[16:31], v[76:79], v[64:67], v[16:31]\n\t" \
  "ds_read_b64_tr_b16 v[88:89], v106 offset:3072\n\t" \
  "ds_read_b64_tr_b16 v[90:91], v106 offset:3840\n\t" \
  "s_cbranch_vccz LP_d5_%=\n\t" \
  "buffer_load_dwordx4 v110, %[rw], s41 offen offset:0 lds\n\t" \
  "LP_d5_%=:\n\t" \
  "v_mfma_f32_32x32x16_bf16 v[32:47], v[72:75], v[68:71], v[32:47]\n\t" \
  "ds_read_b64_tr_b16 v[92:93], v107 offset:3072\n\t" \
  "ds_read_b64_tr_b16 v[94:95], v107 offset:3840\n\t" \
  "v_mfma_f32_32x32x16_bf16 v[48:63], v[76:79], v[68:71], v[48:63]\n\t" \
  "s_cbranch_vccz LP_d6_%=\n\t" \
  "buffer_load_dwordx4 v111, %[rw], s41 offen offset:1024 lds\n\t" \
  "LP_d6_%=:\n\t" \
  "s_waitcnt lgkmcnt(0)\n\t" \
  "v_mfma_f32_32x32x16_bf16 v[0:15], v[88:91], v[80:83], v[0:15]\n\t" \
  "ds_read_b128 v[64:67], v102 offset:0\n\t" \
  "ds_read_b128 v[68:71], v102 offset:4096\n\t" \
  "v_mfma_f32_32x32x16_bf16 v[16:31], v[92:95], v[80:83], v[16:31]\n\t" \
  "ds_read_b64_tr_b16 v[72:73], v106 offset:6144\n\t" \
  "ds_read_b64_tr_b16 v[74:75], v106 offset:6912\n\t" \
  "s_cbranch_vccz LP_d7_%=\n\t" \
  "buffer_load_dwordx4 v112, %[rw], s41 offen offset:2048 lds\n\t" \
  "LP_d7_%=:\n\t" \
  "s_add_u32 m0, %[lq], 46080\n\t" \
  "s_and_b64 vcc, vcc, s[52:53]\n\t" \
  "v_mfma_f32_32x32x16_bf16 v[32:47], v[88:91], v[84:87], v[32:47]\n\t" \
  "ds_read_b64_tr_b16 v[76:77], v107 offset:6144\n\t" \
  "ds_read_b64_tr_b16 v[78:79], v107 offset:6912\n\t" \
  "v_mfma_f32_32x32x16_bf16 v[48:63], v[92:95], v[84:87], v[48:63]\n\t" \
  "s_cbranch_vccz LP_d8_%=\n\t" \
  "buffer_load_dwordx4 v113, %[rq], s42 offen lds\n\t" \
  "LP_d8_%=:\n\t" \
  "s_add_u32 s50, s50, 1\n\t" \
  "s_cmp_lt_u32 s50, 8\n\t" \
  "s_cselect_b64 vcc, -1, 0\n\t" \
  "s_waitcnt lgkmcnt(0)\n\t" \
  "v_mfma_f32_32x32x16_bf16 v[0:15], v[72:75], v[64:67], v[0:15]\n\t" \
  "ds_read_b128 v[80:83], v103 offset:0\n\t" \
  "ds_read_b128 v[84:87], v103 offset:4096\n\t" \
  "ds_read_b64_tr_b16 v[88:89], v106 offset:9216\n\t" \
  "ds_read_b64_tr_b16 v[90:91], v106 offset:9984\n\t" \
  "v_mfma_f32_32x32x16_bf16 v[16:31], v[76:79], v[64:67], v[16:31]\n\t" \
  "ds_read_b64_tr_b16 v[92:93], v107 offset:9216\n\t" \
  "ds_read_b64_tr_b16 v[94:95], v107 offset:9984\n\t" \
  "s_add_u32 s46, s46, 1\n\t" \
  "s_cmp_eq_u32 s46, 10\n\t" \
  "s_cselect_b32 s46, 0, s46\n\t" \
  "v_mfma_f32_32x32x16_bf16 v[32:47], v[72:75], v[68:71], v[32:47]\n\t" \
  "s_lshl_b32 s40, s46, 7\n\t" \
  "s_mul_i32 s41, s46, 196608\n\t" \
  "s_mul_i32 s42, s46, 81920\n\t" \
  "s_add_u32 m0, %[lx], 92160\n\t" \
  "v_mfma_f32_32x32x16_bf16 v[48:63], v[76:79], v[68:71], v[48:63]\n\t" \
  "s_cbranch_vccnz LP_u_w_%=\n\t" \
  "s_cmp_eq_u32 %[dp], 8\n\t" \
  "s_cbranch_scc1 LP_u_w_%=\n\t" \
  "s_waitcnt vmcnt(0)\n\t" \
  "LP_u_w_%=:\n\t" \
  "s_waitcnt vmcnt(5) lgkmcnt(0)\n\t" \
  "s_barrier\n\t" \
  "v_mfma_f32_32x32x16_bf16 v[0:15], v[88:91], v[80:83], v[0:15]\n\t" \
  "ds_read_b128 v[64:67], v96 offset:0\n\t" \
  "ds_read_b128 v[68:71], v96 offset:4096\n\t" \
  "v_mfma_f32_32x32x16_bf16 v[16:31], v[92:95], v[80:83], v[16:31]\n\t" \
  "ds_read_b64_tr_b16 v[72:73], v104 offset:0\n\t" \
  "ds_read_b64_tr_b16 v[74:75], v104 offset:768\n\t" \
  "s_cbranch_vccz LP_d9_%=\n\t" \
  "buffer_load_dwordx4 v108, %[rx], s40 offen offset:0 lds\n\t" \
  "LP_d9_%=:\n\t" \
  "v_mfma_f32_32x32x16_bf16 v[32:47], v[88:91], v[84:87], v[32:47]\n\t" \
  "ds_read_b64_tr_b16 v[76:77], v105 offset:0\n\t" \
  "ds_read_b64_tr_b16 v[78:79], v105 offset:768\n\t" \
  "v_mfma_f32_32x32x16_bf16 v[48:63], v[92:95], v[84:87], v[48:63]\n\t" \
  "s_cbranch_vccz LP_d10_%=\n\t" \
  "buffer_load_dwordx4 v109, %[rx], s40 offen offset:1024 lds\n\t" \
  "LP_d10_%=:\n\t" \
  "s_add_u32 m0, %[lw], 92160\n\t" \
  "s_waitcnt lgkmcnt(0)\n\t" \
  "v_mfma_f32_32x32x16_bf16 v[0:15], v[72:75], v[64:67], v[0:15]\n\t" \
  "ds_read_b128 v[80:83], v97 offset:0\n\t" \
  "ds_read_b128 v[84:87], v97 offset:4096\n\t" \
  "v_mfma_f32_32x32x16_bf16 v[16:31], v[76:79], v[64:67], v[16:31]\n\t" \
  "ds_read_b64_tr_b16 v[88:89], v104 offset:3072\n\t" \
  "ds_read_b64_tr_b16 v[90:91], v104 offset:3840\n\t" \
  "s_cbranch_vccz LP_d11_%=\n\t" \
  "buffer_load_dwordx4 v110, %[rw], s41 offen offset:0 lds\n\t" \
  "LP_d11_%=:\n\t" \
  "v_mfma_f32_32x32x16_bf16 v[32:47], v[72:75], v[68:71], v[32:47]\n\t" \
  "ds_read_b64_tr_b16 v[92:93], v105 offset:3072\n\t" \
  "ds_read_b64_tr_b16 v[94:95], v105 offset:3840\n\t" \
  "v_mfma_f32_32x32x16_bf16 v[48:63], v[76:79], v[68:71], v[48:63]\n\t" \
  "s_cbranch_vccz LP_d12_%=\n\t" \
  "buffer_load_dwordx4 v111, %[rw], s41 offen offset:1024 lds\n\t" \
  "LP_d12_%=:\n\t" \
  "s_waitcnt lgkmcnt(0)\n\t" \
  "v_mfma_f32_32x32x16_bf16 v[0:15], v[88:91], v[80:83], v[0:15]\n\t" \
  "ds_read_b128 v[64:67], v98 offset:0\n\t" \
  "ds_read_b128 v[68:71], v98 offset:4096\n\t" \
  "v_mfma_f32_32x32x16_bf16 v[16:31], v[92:95], v[80:83], v[16:31]\n\t" \
  "ds_read_b64_tr_b16 v[72:73], v104 offset:6144\n\t" \
  "ds_read_b64_tr_b16 v[74:75], v104 offset:6912\n\t" \
  "s_cbranch_vccz LP_d13_%=\n\t" \
  "buffer_load_dwordx4 v112, %[rw], s41 offen offset:2048 lds\n\t" \
  "LP_d13_%=:\n\t" \
  "s_add_u32 m0, %[lq], 92160\n\t" \
  "s_and_b64 vcc, vcc, s[52:53]\n\t" \
  "v_mfma_f32_32x32x16_bf16 v[32:47], v[88:91], v[84:87], v[32:47]\n\t" \
  "ds_read_b64_tr_b16 v[76:77], v105 offset:6144\n\t" \
  "ds_read_b64_tr_b16 v[78:79], v105 offset:6912\n\t" \
  "v_mfma_f32_32x32x16_bf16 v[48:63], v[92:95], v[84:87], v[48:63]\n\t" \
  "s_cbranch_vccz LP_d14_%=\n\t" \
  "buffer_load_dwordx4 v113, %[rq], s42 offen lds\n\t" \
  "LP_d14_%=:\n\t" \
  "s_add_u32 s50, s50, 1\n\t" \
  "s_cmp_lt_u32 s50, 8\n\t" \
  "s_cselect_b64 vcc, -1, 0\n\t" \
  "s_cmp_lt_u32 s50, 10\n\t" \
  "s_cbranch_scc1 LP_u_loop_%=\n\t" \
  "s_waitcnt lgkmcnt(0)\n\t" \
  "v_mfma_f32_32x32x16_bf16 v[0:15], v[72:75], v[64:67], v[0:15]\n\t" \
  "ds_read_b128 v[80:83], v99 offset:0\n\t" \
  "ds_read_b128 v[84:87], v99 offset:4096\n\t" \
  "ds_read_b64_tr_b16 v[88:89], v104 offset:9216\n\t" \
  "ds_read_b64_tr_b16 v[90:91], v104 offset:9984\n\t" \
  "v_mfma_f32_32x32x16_bf16 v[16:31], v[76:79], v[64:67], v[16:31]\n\t" \
  "ds_read_b64_tr_b16 v[92:93], v105 offset:9216\n\t" \
  "ds_read_b64_tr_b16 v[94:95], v105 offset:9984\n\t" \
  "s_add_u32 s46, s46, 1\n\t" \
  "s_cmp_eq_u32 s46, 10\n\t" \
  "s_cselect_b32 s46, 0, s46\n\t" \
  "v_mfma_f32_32x32x16_bf16 v[32:47], v[72:75], v[68:71], v[32:47]\n\t" \
  "s_lshl_b32 s40, s46, 7\n\t" \
  "s_mul_i32 s41, s46, 196608\n\t" \
  "s_mul_i32 s42, s46, 81920\n\t" \
  "s_add_u32 m0, %[lx], 0\n\t" \
  "v_mfma_f32_32x32x16_bf16 v[48:63], v[76:79], v[68:71], v[48:63]\n\t" \
  "s_branch LP_end_%=\n\t" \
  "LP_pub_%=:\n\t" \
  "s_mov_b64 s[48:49], exec\n\t" \
  "s_mov_b64 exec, 0xff\n\t" \
  "global_store_dwordx2 v114, v[116:117], %[wsb] sc1\n\t" \
  "s_mov_b64 exec, s[48:49]\n\t" \
  "s_branch LP_pubd_%=\n\t" \
  "LP_q_in_%=:\n\t" \
  "ds_read_b128 v[64:67], v96 offset:0\n\t" \
  "ds_read_b128 v[68:71], v96 offset:4096\n\t" \
  "ds_read_b64_tr_b16 v[72:73], v104 offset:0\n\t" \
  "ds_read_b64_tr_b16 v[74:75], v104 offset:320\n\t" \
  "ds_read_b64_tr_b16 v[76:77], v105 offset:0\n\t" \
  "ds_read_b64_tr_b16 v[78:79], v105 offset:320\n\t" \
  "s_mov_b64 vcc, s[52:53]\n\t" \
  "s_add_u32 m0, %[lx], 92160\n\t" \
  "s_nop 0\n\t" \
  "buffer_load_dwordx4 v108, %[rx], s40 offen offset:0 lds\n\t" \
  "buffer_load_dwordx4 v109, %[rx], s40 offen offset:1024 lds\n\t" \
  "s_add_u32 m0, %[lw], 92160\n\t" \
  "s_waitcnt lgkmcnt(0)\n\t" \
  "v_mfma_f32_32x32x16_bf16 v[0:15], v[72:75], v[64:67], 0\n\t" \
  "ds_read_b128 v[80:83], v97 offset:0\n\t" \
  "ds_read_b128 v[84:87], v97 offset:4096\n\t" \
  "v_mfma_f32_32x32x16_bf16 v[16:31], v[76:79], v[64:67], 0\n\t" \
  "ds_read_b64_tr_b16 v[88:89], v104 offset:1280\n\t" \
  "ds_read_b64_tr_b16 v[90:91], v104 offset:1600\n\t" \
  "buffer_load_dwordx4 v110, %[rw], s41 offen offset:0 lds\n\t" \
  "v_mfma_f32_32x32x16_bf16 v[32:47], v[72:75], v[68:71], 0\n\t" \
  "ds_read_b64_tr_b16 v[92:93], v105 offset:1280\n\t" \
  "ds_read_b64_tr_b16 v[94:95], v105 offset:1600\n\t" \
  "v_mfma_f32_32x32x16_bf16 v[48:63], v[76:79], v[68:71], 0\n\t" \
  "buffer_load_dwordx4 v111, %[rw], s41 offen offset:1024 lds\n\t" \
  "s_waitcnt lgkmcnt(0)\n\t" \
  "v_mfma_f32_32x32x16_bf16 v[0:15], v[88:91], v[80:83], v[0:15]\n\t" \
  "ds_read_b128 v[64:67], v98 offset:0\n\t" \
  "ds_read_b128 v[68:71], v98 offset:4096\n\t" \
  "v_mfma_f32_32x32x16_bf16 v[16:31], v[92:95], v[80:83], v[16:31]\n\t" \
  "ds_read_b64_tr_b16 v[72:73], v104 offset:2560\n\t" \
  "ds_read_b64_tr_b16 v[74:75], v104 offset:2880\n\t" \
  "buffer_load_dwordx4 v112, %[rw], s41 offen offset:2048 lds\n\t" \
  "s_add_u32 m0, %[lq], 92160\n\t" \
  "v_mfma_f32_32x32x16_bf16 v[32:47], v[88:91], v[84:87], v[32:47]\n\t" \
  "ds_read_b64_tr_b16 v[76:77], v105 offset:2560\n\t" \
  "ds_read_b64_tr_b16 v[78:79], v105 offset:2880\n\t" \
  "v_mfma_f32_32x32x16_bf16 v[48:63], v[92:95], v[84:87], v[48:63]\n\t" \
  "s_cbranch_vccz LP_d15_%=\n\t" \
  "buffer_load_dwordx4 v113, %[rq], s42 offen lds\n\t" \
  "LP_d15_%=:\n\t" \
  "s_add_u32 s50, s50, 1\n\t" \
  "s_cmp_lt_u32 s50, 8\n\t" \
  "s_cselect_b64 vcc, -1, 0\n\t" \
  "LP_q_loop_%=:\n\t" \
  "s_waitcnt lgkmcnt(0)\n\t" \
  "v_mfma_f32_32x32x16_bf16 v[0:15], v[72:75], v[64:67], v[0:15]\n\t" \
  "ds_read_b128 v[80:83], v99 offset:0\n\t" \
  "ds_read_b128 v[84:87], v99 offset:4096\n\t" \
  "ds_read_b64_tr_b16 v[88:89], v104 offset:3840\n\t" \
  "ds_read_b64_tr_b16 v[90:91], v104 offset:4160\n\t" \
  "v_mfma_f32_32x32x16_bf16 v[16:31], v[76:79], v[64:67], v[16:31]\n\t" \
  "ds_read_b64_tr_b16 v[92:93], v105 offset:3840\n\t" \
  "ds_read_b64_tr_b16 v[94:95], v105 offset:4160\n\t" \
  "s_add_u32 s46, s46, 1\n\t" \
  "s_cmp_eq_u32 s46, 10\n\t" \
  "s_cselect_b32 s46, 0, s46\n\t" \
  "v_mfma_f32_32x32x16_bf16 v[32:47], v[72:75], v[68:71], v[32:47]\n\t" \
  "s_lshl_b32 s40, s46, 7\n\t" \
  "s_mul_i32 s41, s46, 196608\n\t" \
  "s_mul_i32 s42, s46, 81920\n\t" \
  "s_add_u32 m0, %[lx], 0\n\t" \
  "v_mfma_f32_32x32x16_bf16 v[48:63], v[76:79], v[68:71], v[48:63]\n\t" \
  "s_waitcnt vmcnt(5) lgkmcnt(0)\n\t" \
  "s_barrier\n\t" \
  "v_mfma_f32_32x32x16_bf16 v[0:15], v[88:91], v[80:83], v[0:15]\n\t" \
  "ds_read_b128 v[64:67], v96 offset:46080\n\t" \
  "ds_read_b128 v[68:71], v96 offset:50176\n\t" \
  "v_mfma_f32_32x32x16_bf16 v[16:31], v[92:95], v[80:83], v[16:31]\n\t" \
  "ds_read_b64_tr_b16 v[72:73], v104 offset:46080\n\t" \
  "ds_read_b64_tr_b16 v[74:75], v104 offset:46400\n\t" \
  "buffer_load_dwordx4 v108, %[rx], s40 offen offset:0 lds\n\t" \
  "v_mfma_f32_32x32x16_bf16 v[32:47], v[88:91], v[84:87], v[32:47]\n\t" \
  "ds_read_b64_tr_b16 v[76:77], v105 offset:46080\n\t" \
  "ds_read_b64_tr_b16 v[78:79], v105 offset:46400\n\t" \
  "v_mfma_f32_32x32x16_bf16 v[48:63], v[92:95], v[84:87], v[48:63]\n\t" \
  "buffer_load_dwordx4 v109, %[rx], s40 offen offset:1024 lds\n\t" \
  "s_add_u32 m0, %[lw], 0\n\t" \
  "s_waitcnt lgkmcnt(0)\n\t" \
  "v_mfma_f32_32x32x16_bf16 v[0:15], v[72:75], v[64:67], v[0:15]\n\t" \
  "ds_read_b128 v[80:83], v97 offset:46080\n\t" \
  "ds_read_b128 v[84:87], v97 offset:50176\n\t" \
  "v_mfma_f32_32x32x16_bf16 v[16:31], v[76:79], v[64:67], v[16:31]\n\t" \
  "ds_read_b64_tr_b16 v[88:89], v104 offset:47360\n\t" \
  "ds_read_b64_tr_b16 v[90:91], v104 offset:47680\n\t" \
  "buffer_load_dwordx4 v110, %[rw], s41 offen offset:0 lds\n\t" \
  "v_mfma_f32_32x32x16_bf16 v[32:47], v[72:75], v[68:71], v[32:47]\n\t" \
  "ds_read_b64_tr_b16 v[92:93], v105 offset:47360\n\t" \
  "ds_read_b64_tr_b16 v[94:95], v105 offset:47680\n\t" \
  "v_mfma_f32_32x32x16_bf16 v[48:63], v[76:79], v[68:71], v[48:63]\n\t" \
  "buffer_load_dwordx4 v111, %[rw], s41 offen offset:1024 lds\n\t" \
  "s_waitcnt lgkmcnt(0)\n\t" \
  "v_mfma_f32_32x32x16_bf16 v[0:15], v[88:91], v[80:83], v[0:15]\n\t" \
  "ds_read_b128 v[64:67], v98 offset:46080\n\t" \
  "ds_read_b128 v[68:71], v98 offset:50176\n\t" \
  "v_mfma_f32_32x32x16_bf16 v[16:31], v[92:95], v[80:83], v[16:31]\n\t" \
  "ds_read_b64_tr_b16 v[72:73], v104 offset:48640\n\t" \
  "ds_read_b64_tr_b16 v[74:75], v104 offset:48960\n\t" \
  "buffer_load_dwordx4 v112, %[rw], s41 offen offset:2048 lds\n\t" \
  "s_add_u32 m0, %[lq], 0\n\t" \
  "s_and_b64 vcc, vcc, s[52:53]\n\t" \
  "v_mfma_f32_32x32x16_bf16 v[32:47], v[88:91], v[84:87], v[32:47]\n\t" \
  "ds_read_b64_tr_b16 v[76:77], v105 offset:48640\n\t" \
  "ds_read_b64_tr_b16 v[78:79], v105 offset:48960\n\t" \
  "v_mfma_f32_32x32x16_bf16 v[48:63], v[92:95], v[84:87], v[48:63]\n\t" \
  "s_cbranch_vccz LP_d16_%=\n\t" \
  "buffer_load_dwordx4 v113, %[rq], s42 offen lds\n\t" \
  "LP_d16_%=:\n\t" \
  "s_cmp_eq_u32 s50, 7\n\t" \
  "s_cbranch_scc0 LP_d17_%=\n\t" \
  "global_load_dwordx4 v[136:139], v126, %[wsb] sc1\n\t" \
  "global_load_dwordx4 v[140:143], v126, %[wsb] offset:16 sc1\n\t" \
  "LP_d17_%=:\n\t" \
  "s_add_u32 s50, s50, 1\n\t" \
  "s_cmp_lt_u32 s50, 8\n\t" \
  "s_cselect_b64 vcc, -1, 0\n\t" \
  "s_waitcnt lgkmcnt(0)\n\t" \
  "v_mfma_f32_32x32x16_bf16 v[0:15], v[72:75], v[64:67], v[0:15]\n\t" \
  "ds_read_b128 v[80:83], v99 offset:46080\n\t" \
  "ds_read_b128 v[84:87], v99 offset:50176\n\t" \
  "ds_read_b64_tr_b16 v[88:89], v104 offset:49920\n\t" \
  "ds_read_b64_tr_b16 v[90:91], v104 offset:50240\n\t" \
  "v_mfma_f32_32x32x16_bf16 v[16:31], v[76:79], v[64:67], v[16:31]\n\t" \
  "ds_read_b64_tr_b16 v[92:93], v105 offset:49920\n\t" \
  "ds_read_b64_tr_b16 v[94:95], v105 offset:50240\n\t" \
  "s_add_u32 s46, s46, 1\n\t" \
  "s_cmp_eq_u32 s46, 10\n\t" \
  "s_cselect_b32 s46, 0, s46\n\t" \
  "v_mfma_f32_32x32x16_bf16 v[32:47], v[72:75], v[68:71], v[32:47]\n\t" \
  "s_lshl_b32 s40, s46, 7\n\t" \
  "s_mul_i32 s41, s46, 196608\n\t" \
  "s_mul_i32 s42, s46, 81920\n\t" \
  "s_add_u32 m0, %[lx], 46080\n\t" \
  "v_mfma_f32_32x32x16_bf16 v[48:63], v[76:79], v[68:71], v[48:63]\n\t" \
  "s_waitcnt vmcnt(5) lgkmcnt(0)\n\t" \
  "s_barrier\n\t" \
  "v_mfma_f32_32x32x16_bf16 v[0:15], v[88:91], v[80:83], v[0:15]\n\t" \
  "ds_read_b128 v[64:67], v100 offset:0\n\t" \
  "ds_read_b128 v[68:71], v100 offset:4096\n\t" \
  "v_mfma_f32_32x32x16_bf16 v[16:31], v[92:95], v[80:83], v[16:31]\n\t" \
  "ds_read_b64_tr_b16 v[72:73], v106 offset:0\n\t" \
  "ds_read_b64_tr_b16 v[74:75], v106 offset:320\n\t" \
  "s_cbranch_vccz LP_d18_%=\n\t" \
  "buffer_load_dwordx4 v108, %[rx], s40 offen offset:0 lds\n\t" \
  "LP_d18_%=:\n\t" \
  "v_mfma_f32_32x32x16_bf16 v[32:47], v[88:91], v[84:87], v[32:47]\n\t" \
  "ds_read_b64_tr_b16 v[76:77], v107 offset:0\n\t" \
  "ds_read_b64_tr_b16 v[78:79], v107 offset:320\n\t" \
  "v_mfma_f32_32x32x16_bf16 v[48:63], v[92:95], v[84:87], v[48:63]\n\t" \
  "s_cbranch_vccz LP_d19_%=\n\t" \
  "buffer_load_dwordx4 v109, %[rx], s40 offen offset:1024 lds\n\t" \
  "LP_d19_%=:\n\t" \
  "s_add_u32 m0, %[lw], 46080\n\t" \
  "s_waitcnt lgkmcnt(0)\n\t" \
  "v_mfma_f32_32x32x16_bf16 v[0:15], v[72:75], v[64:67], v[0:15]\n\t" \
  "ds_read_b128 v[80:83], v101 offset:0\n\t" \
  "ds_read_b128 v[84:87], v101 offset:4096\n\t" \
  "v_mfma_f32_32x32x16_bf16 v[16:31], v[76:79], v[64:67], v[16:31]\n\t" \
  "ds_read_b64_tr_b16 v[88:89], v106 offset:1280\n\t" \
  "ds_read_b64_tr_b16 v[90:91], v106 offset:1600\n\t" \
  "s_cbranch_vccz LP_d20_%=\n\t" \
  "buffer_load_dwordx4 v110, %[rw], s41 offen offset:0 lds\n\t" \
  "LP_d20_%=:\n\t" \
  "v_mfma_f32_32x32x16_bf16 v[32:47], v[72:75], v[68:71], v[32:47]\n\t" \
  "ds_read_b64_tr_b16 v[92:93], v107 offset:1280\n\t" \
  "ds_read_b64_tr_b16 v[94:95], v107 offset:1600\n\t" \
  "v_mfma_f32_32x32x16_bf16 v[48:63], v[76:79], v[68:71], v[48:63]\n\t" \
  "s_cbranch_vccz LP_d21_%=\n\t" \
  "buffer_load_dwordx4 v111, %[rw], s41 offen offset:1024 lds\n\t" \
  "LP_d21_%=:\n\t" \
  "s_waitcnt lgkmcnt(0)\n\t" \
  "v_mfma_f32_32x32x16_bf16 v[0:15], v[88:91], v[80:83], v[0:15]\n\t" \
  "ds_read_b128 v[64:67], v102 offset:0\n\t" \
  "ds_read_b128 v[68:71], v102 offset:4096\n\t" \
  "v_mfma_f32_32x32x16_bf16 v[16:31], v[92:95], v[80:83], v[16:31]\n\t" \
  "ds_read_b64_tr_b16 v[72:73], v106 offset:2560\n\t" \
  "ds_read_b64_tr_b16 v[74:75], v106 offset:2880\n\t" \
  "s_cbranch_vccz LP_d22_%=\n\t" \
  "buffer_load_dwordx4 v112, %[rw], s41 offen offset:2048 lds\n\t" \
  "LP_d22_%=:\n\t" \
  "s_add_u32 m0, %[lq], 46080\n\t" \
  "s_and_b64 vcc, vcc, s[52:53]\n\t" \
  "v_mfma_f32_32x32x16_bf16 v[32:47], v[88:91], v[84:87], v[32:47]\n\t" \
  "ds_read_b64_tr_b16 v[76:77], v107 offset:2560\n\t" \
  "ds_read_b64_tr_b16 v[78:79], v107 offset:2880\n\t" \
  "v_mfma_f32_32x32x16_bf16 v[48:63], v[92:95], v[84:87], v[48:63]\n\t" \
  "s_cbranch_vccz LP_d23_%=\n\t" \
  "buffer_load_dwordx4 v113, %[rq], s42 offen lds\n\t" \
  "LP_d23_%=:\n\t" \
  "s_add_u32 s50, s50, 1\n\t" \
  "s_cmp_lt_u32 s50, 8\n\t" \
  "s_cselect_b64 vcc, -1, 0\n\t" \
  "s_waitcnt lgkmcnt(0)\n\t" \
  "v_mfma_f32_32x32x16_bf16 v[0:15], v[72:75], v[64:67], v[0:15]\n\t" \
  "ds_read_b128 v[80:83], v103 offset:0\n\t" \
  "ds_read_b128 v[84:87], v103 offset:4096\n\t" \
  "ds_read_b64_tr_b16 v[88:89], v106 offset:3840\n\t" \
  "ds_read_b64_tr_b16 v[90:91], v106 offset:4160\n\t" \
  "v_mfma_f32_32x32x16_bf16 v[16:31], v[76:79], v[64:67], v[16:31]\n\t" \
  "ds_read_b64_tr_b16 v[92:93], v107 offset:3840\n\t" \
  "ds_read_b64_tr_b16 v[94:95], v107 offset:4160\n\t" \
  "s_add_u32 s46, s46, 1\n\t" \
  "s_cmp_eq_u32 s46, 10\n\t" \
  "s_cselect_b32 s46, 0, s46\n\t" \
  "v_mfma_f32_32x32x16_bf16 v[32:47], v[72:75], v[68:71], v[32:47]\n\t" \
  "s_lshl_b32 s40, s46, 7\n\t" \
  "s_mul_i32 s41, s46, 196608\n\t" \
  "s_mul_i32 s42, s46, 81920\n\t" \
  "s_add_u32 m0, %[lx], 92160\n\t" \
  "v_mfma_f32_32x32x16_bf16 v[48:63], v[76:79], v[68:71], v[48:63]\n\t" \
  "s_cbranch_vccnz LP_q_w_%=\n\t" \
  "s_waitcnt vmcnt(0)\n\t" \
  "LP_q_w_%=:\n\t" \
  "s_waitcnt vmcnt(5) lgkmcnt(0)\n\t" \
  "s_barrier\n\t" \
  "v_mfma_f32_32x32x16_bf16 v[0:15], v[88:91], v[80:83], v[0:15]\n\t" \
  "ds_read_b128 v[64:67], v96 offset:0\n\t" \
  "ds_read_b128 v[68:71], v96 offset:4096\n\t" \
  "v_mfma_f32_32x32x16_bf16 v[16:31], v[92:95], v[80:83], v[16:31]\n\t" \
  "ds_read_b64_tr_b16 v[72:73], v104 offset:0\n\t" \
  "ds_read_b64_tr_b16 v[74:75], v104 offset:320\n\t" \
  "s_cmp_eq_u32 s50, 9\n\t" \
  "s_cbranch_scc0 LP_k0_%=\n\t" \
  "s_mov_b64 s[48:49], 0\n\t" \
  "v_lshrrev_b64 v[132:133], 24, v[136:137]\n\t" \
  "v_cmp_ne_u64 vcc, %[t40], v[132:133]\n\t" \
  "s_or_b64 s[48:49], vcc, s[48:49]\n\t" \
  "v_lshrrev_b64 v[132:133], 24, v[138:139]\n\t" \
  "v_cmp_ne_u64 vcc, %[t40], v[132:133]\n\t" \
  "s_or_b64 s[48:49], vcc, s[48:49]\n\t" \
  "v_lshrrev_b64 v[132:133], 24, v[140:141]\n\t" \
  "v_cmp_ne_u64 vcc, %[t40], v[132:133]\n\t" \
  "s_or_b64 s[48:49], vcc, s[48:49]\n\t" \
  "v_lshrrev_b64 v[132:133], 24, v[142:143]\n\t" \
  "v_cmp_ne_u64 vcc, %[t40], v[132:133]\n\t" \
  "s_or_b64 s[48:49], vcc, s[48:49]\n\t" \
  "s_mov_b64 vcc, s[48:49]\n\t" \
  "s_cbranch_vccnz LP_k1_%=\n\t" \
  "s_mov_b32 m0, %[lk]\n\t" \
  "s_mov_b64 s[48:49], %[gk]\n\t" \
  "s_mov_b32 s51, 1\n\t" \
  "global_load_lds_dwordx4 v127, s[48:49] offset:0 sc1\n\t" \
  "global_load_lds_dwordx4 v127, s[48:49] offset:1024 sc1\n\t" \
  "global_load_lds_dwordx4 v127, s[48:49] offset:2048 sc1\n\t" \
  "global_load_lds_dwordx4 v128, s[48:49] offset:3072 sc1\n\t" \
  "s_add_u32 m0, m0, 4096\n\t" \
  "s_add_u32 s48, s48, 57344\n\t" \
  "s_addc_u32 s49, s49, 0\n\t" \
  "global_load_lds_dwordx4 v127, s[48:49] offset:0 sc1\n\t" \
  "global_load_lds_dwordx4 v127, s[48:49] offset:1024 sc1\n\t" \
  "global_load_lds_dwordx4 v127, s[48:49] offset:2048 sc1\n\t" \
  "global_load_lds_dwordx4 v128, s[48:49] offset:3072 sc1\n\t" \
  "LP_k1_%=:\n\t" \
  "s_mov_b64 vcc, 0\n\t" \
  "LP_k0_%=:\n\t" \
  "s_cbranch_vccz LP_d24_%=\n\t" \
  "buffer_load_dwordx4 v108, %[rx], s40 offen offset:0 lds\n\t" \
  "LP_d24_%=:\n\t" \
  "v_mfma_f32_32x32x16_bf16 v[32:47], v[88:91], v[84:87], v[32:47]\n\t" \
  "ds_read_b64_tr_b16 v[76:77], v105 offset:0\n\t" \
  "ds_read_b64_tr_b16 v[78:79], v105 offset:320\n\t" \
  "v_mfma_f32_32x32x16_bf16 v[48:63], v[92:95], v[84:87], v[48:63]\n\t" \
  "s_cbranch_vccz LP_d25_%=\n\t" \
  "buffer_load_dwordx4 v109, %[rx], s40 offen offset:1024 lds\n\t" \
  "LP_d25_%=:\n\t" \
  "s_add_u32 m0, %[lw], 92160\n\t" \
  "s_waitcnt lgkmcnt(0)\n\t" \
  "v_mfma_f32_32x32x16_bf16 v[0:15], v[72:75], v[64:67], v[0:15]\n\t" \
  "ds_read_b128 v[80:83], v97 offset:0\n\t" \
  "ds_read_b128 v[84:87], v97 offset:4096\n\t" \
  "v_mfma_f32_32x32x16_bf16 v[16:31], v[76:79], v[64:67], v[16:31]\n\t" \
  "ds_read_b64_tr_b16 v[88:89], v104 offset:1280\n\t" \
  "ds_read_b64_tr_b16 v[90:91], v104 offset:1600\n\t" \
  "s_cbranch_vccz LP_d26_%=\n\t" \
  "buffer_load_dwordx4 v110, %[rw], s41 offen offset:0 lds\n\t" \
  "LP_d26_%=:\n\t" \
  "v_mfma_f32_32x32x16_bf16 v[32:47], v[72:75], v[68:71], v[32:47]\n\t" \
  "ds_read_b64_tr_b16 v[92:93], v105 offset:1280\n\t" \
  "ds_read_b64_tr_b16 v[94:95], v105 offset:1600\n\t" \
  "v_mfma_f32_32x32x16_bf16 v[48:63], v[76:79], v[68:71], v[48:63]\n\t" \
  "s_cbranch_vccz LP_d27_%=\n\t" \
  "buffer_load_dwordx4 v111, %[rw], s41 offen offset:1024 lds\n\t" \
  "LP_d27_%=:\n\t" \
  "s_waitcnt lgkmcnt(0)\n\t" \
  "v_mfma_f32_32x32x16_bf16 v[0:15], v[88:91], v[80:83], v[0:15]\n\t" \
  "ds_read_b128 v[64:67], v98 offset:0\n\t" \
  "ds_read_b128 v[68:71], v98 offset:4096\n\t" \
  "v_mfma_f32_32x32x16_bf16 v[16:31], v[92:95], v[80:83], v[16:31]\n\t" \
  "ds_read_b64_tr_b16 v[72:73], v104 offset:2560\n\t" \
  "ds_read_b64_tr_b16 v[74:75], v104 offset:2880\n\t" \
  "s_cbranch_vccz LP_d28_%=\n\t" \
  "buffer_load_dwordx4 v112, %[rw], s41 offen offset:2048 lds\n\t" \
  "LP_d28_%=:\n\t" \
  "s_add_u32 m0, %[lq], 92160\n\t" \
  "s_and_b64 vcc, vcc, s[52:53]\n\t" \
  "v_mfma_f32_32x32x16_bf16 v[32:47], v[88:91], v[84:87], v[32:47]\n\t" \
  "ds_read_b64_tr_b16 v[76:77], v105 offset:2560\n\t" \
  "ds_read_b64_tr_b16 v[78:79], v105 offset:2880\n\t" \
  "v_mfma_f32_32x32x16_bf16 v[48:63], v[92:95], v[84:87], v[48:63]\n\t" \
  "s_cbranch_vccz LP_d29_%=\n\t" \
  "buffer_load_dwordx4 v113, %[rq], s42 offen lds\n\t" \
  "LP_d29_%=:\n\t" \
  "s_add_u32 s50, s50, 1\n\t" \
  "s_cmp_lt_u32 s50, 8\n\t" \
  "s_cselect_b64 vcc, -1, 0\n\t" \
  "s_cmp_lt_u32 s50, 10\n\t" \
  "s_cbranch_scc1 LP_q_loop_%=\n\t" \
  "s_waitcnt lgkmcnt(0)\n\t" \
  "v_mfma_f32_32x32x16_bf16 v[0:15], v[72:75], v[64:67], v[0:15]\n\t" \
  "ds_read_b128 v[80:83], v99 offset:0\n\t" \
  "ds_read_b128 v[84:87], v99 offset:4096\n\t" \
  "ds_read_b64_tr_b16 v[88:89], v104 offset:3840\n\t" \
  "ds_read_b64_tr_b16 v[90:91], v104 offset:4160\n\t" \
  "v_mfma_f32_32x32x16_bf16 v[16:31], v[76:79], v[64:67], v[16:31]\n\t" \
  "ds_read_b64_tr_b16 v[92:93], v105 offset:3840\n\t" \
  "ds_read_b64_tr_b16 v[94:95], v105 offset:4160\n\t" \
  "s_add_u32 s46, s46, 1\n\t" \
  "s_cmp_eq_u32 s46, 10\n\t" \
  "s_cselect_b32 s46, 0, s46\n\t" \
  "v_mfma_f32_32x32x16_bf16 v[32:47], v[72:75], v[68:71], v[32:47]\n\t" \
  "s_lshl_b32 s40, s46, 7\n\t" \
  "s_mul_i32 s41, s46, 196608\n\t" \
  "s_mul_i32 s42, s46, 81920\n\t" \
  "s_add_u32 m0, %[lx], 0\n\t" \
  "v_mfma_f32_32x32x16_bf16 v[48:63], v[76:79], v[68:71], v[48:63]\n\t" \
  "LP_end_%=:\n\t" \
  "s_waitcnt lgkmcnt(0)\n\t" \
  "v_mfma_f32_32x32x16_bf16 v[0:15], v[88:91], v[80:83], v[0:15]\n\t" \
  "v_mfma_f32_32x32x16_bf16 v[16:31], v[92:95], v[80:83], v[16:31]\n\t" \
  "v_mfma_f32_32x32x16_bf16 v[32:47], v[88:91], v[84:87], v[32:47]\n\t" \
  "v_mfma_f32_32x32x16_bf16 v[48:63], v[92:95], v[84:87], v[48:63]\n\t" \
  "s_mov_b32 %[k0], s51\n\t" \
  "s_nop 14\n\t"
#define P1_CLOB "v64", "v65", "v66", "v67", "v68", "v69", "v70", "v71", "v72", "v73", "v74", "v75", "v76", "v77", "v78", "v79", "v80", "v81", "v82", "v83", "v84", "v85", "v86", "v87", "v88", "v89", "v90", "v91", "v92", "v93", "v94", "v95", "v132", "v133", "v134", "v135", "s40", "s41", "s42", "s43", "s44", "s45", "s46", "s47", "s48", "s49", "s50", "s51", "s52", "s53", "vcc", "scc", "memory"
#define ATT_ASM \
  "s_mov_b32 s40, %[gb]\n\t" \
  "s_add_u32 s41, %[kb], 57344\n\t" \
  "s_mov_b32 s46, %[ga]\n\t" \
  "s_mov_b32 s48, %[gw]\n\t" \
  "s_mov_b64 s[52:53], %[tag]\n\t" \
  "v_mov_b32 v68, 0\n\t" \
  "v_mov_b32 v69, 0\n\t" \
  "v_mov_b32 v136, 0xdddddddd\n\t" \
  "s_nop 0\n\t" \
  "v_mfma_f32_32x32x8_bf16 v[0:15], v[68:69], v[68:69], 0\n\t" \
  "v_mfma_f32_32x32x8_bf16 v[16:31], v[68:69], v[68:69], 0\n\t" \
  "v_mfma_f32_32x32x8_bf16 v[32:47], v[68:69], v[68:69], 0\n\t" \
  "v_mov_b32 v48, 0\n\t" \
  "v_mov_b32 v49, 0\n\t" \
  "v_mov_b32 v50, 0\n\t" \
  "v_mov_b32 v51, 0\n\t" \
  "s_waitcnt lgkmcnt(0)\n\t" \
  "s_barrier\n\t" \
  "ds_read_b128 v[108:111], v60\n\t" \
  "ds_read_b64 v[112:113], v61\n\t" \
  "ds_read_b128 v[84:87], v52\n\t" \
  "ds_read_b64 v[88:89], v56\n\t" \
  "buffer_load_dwordx4 v[122:125], v236, %[rs], s40 offen sc1\n\t" \
  "buffer_load_dwordx2 v[126:127], v237, %[rs], s40 offen offset:3072 sc1\n\t" \
  "ds_read_b128 v[90:93], v53\n\t" \
  "ds_read_b64 v[94:95], v57\n\t" \
  "ds_read_b128 v[96:99], v54\n\t" \
  "ds_read_b64 v[100:101], v58\n\t" \
  "ds_read_b128 v[102:105], v55\n\t" \
  "ds_read_b64 v[106:107], v59\n\t" \
  "ds_read_b128 v[114:117], v60 offset:1024\n\t" \
  "ds_read_b128 v[118:121], v60 offset:2048\n\t" \
  "s_waitcnt lgkmcnt(8)\n\t" \
  "s_cmp_lt_u32 %[lw], 10240\n\t" \
  "s_cbranch_scc1 LA_p0_%=\n\t" \
  "s_setprio 1\n\t" \
  "LA_p0_%=:\n\t" \
  "v_mfma_f32_32x32x16_bf16 v[52:67], v[108:111], v[84:87], 0\n\t" \
  "v_mfma_f32_32x32x8_bf16 v[52:67], v[112:113], v[88:89], v[52:67]\n\t" \
  "s_nop 6\n\t" \
  "s_waitcnt lgkmcnt(0)\n\t" \
  "v_exp_f32 v52, v52\n\t" \
  "v_exp_f32 v53, v53\n\t" \
  "v_exp_f32 v54, v54\n\t" \
  "v_exp_f32 v55, v55\n\t" \
  "v_exp_f32 v56, v56\n\t" \
  "v_exp_f32 v57, v57\n\t" \
  "v_mfma_f32_32x32x16_bf16 v[68:83], v[108:111], v[90:93], 0\n\t" \
  "v_exp_f32 v58, v58\n\t" \
  "v_exp_f32 v59, v59\n\t" \
  "v_exp_f32 v60, v60\n\t" \
  "v_exp_f32 v61, v61\n\t" \
  "v_mfma_f32_32x32x8_bf16 v[68:83], v[112:113], v[94:95], v[68:83]\n\t" \
  "v_exp_f32 v62, v62\n\t" \
  "v_exp_f32 v63, v63\n\t" \
  "v_exp_f32 v64, v64\n\t" \
  "v_exp_f32 v65, v65\n\t" \
  "v_smfmac_f32_32x32x32_bf16 v[0:15], v[114:117], v[52:59], v136\n\t" \
  "buffer_load_dwordx4 v[128:131], v238, %[rs], s40 offen offset:1024 sc1\n\t" \
  "buffer_load_dwordx4 v[132:135], v238, %[rs], s40 offen offset:2048 sc1\n\t" \
  "s_add_u32 s40, s40, 3584\n\t" \
  "s_cmp_eq_u32 s40, s41\n\t" \
  "s_cselect_b32 s40, %[kb], s40\n\t" \
  "v_exp_f32 v66, v66\n\t" \
  "v_exp_f32 v67, v67\n\t" \
  "v_exp_f32 v68, v68\n\t" \
  "v_exp_f32 v69, v69\n\t" \
  "v_smfmac_f32_32x32x32_bf16 v[0:15], v[118:121], v[60:67], v136\n\t" \
  "v_exp_f32 v70, v70\n\t" \
  "v_exp_f32 v71, v71\n\t" \
  "v_exp_f32 v72, v72\n\t" \
  "v_exp_f32 v73, v73\n\t" \
  "v_mfma_f32_32x32x16_bf16 v[52:67], v[108:111], v[96:99], 0\n\t" \
  "v_exp_f32 v74, v74\n\t" \
  "v_exp_f32 v75, v75\n\t" \
  "v_exp_f32 v76, v76\n\t" \
  "v_exp_f32 v77, v77\n\t" \
  "v_mfma_f32_32x32x8_bf16 v[52:67], v[112:113], v[100:101], v[52:67]\n\t" \
  "v_exp_f32 v78, v78\n\t" \
  "v_exp_f32 v79, v79\n\t" \
  "v_exp_f32 v80, v80\n\t" \
  "v_exp_f32 v81, v81\n\t" \
  "v_smfmac_f32_32x32x32_bf16 v[12:27], v[114:117], v[68:75], v136\n\t" \
  "v_exp_f32 v82, v82\n\t" \
  "v_exp_f32 v83, v83\n\t" \
  "v_exp_f32 v52, v52\n\t" \
  "v_exp_f32 v53, v53\n\t" \
  "v_smfmac_f32_32x32x32_bf16 v[12:27], v[118:121], v[76:83], v136\n\t" \
  "v_exp_f32 v54, v54\n\t" \
  "v_exp_f32 v55, v55\n\t" \
  "v_exp_f32 v56, v56\n\t" \
  "v_exp_f32 v57, v57\n\t" \
  "v_mfma_f32_32x32x16_bf16 v[68:83], v[108:111], v[102:105], 0\n\t" \
  "v_exp_f32 v58, v58\n\t" \
  "v_exp_f32 v59, v59\n\t" \
  "v_exp_f32 v60, v60\n\t" \
  "v_exp_f32 v61, v61\n\t" \
  "v_mfma_f32_32x32x8_bf16 v[68:83], v[112:113], v[106:107], v[68:83]\n\t" \
  "v_exp_f32 v62, v62\n\t" \
  "v_exp_f32 v63, v63\n\t" \
  "v_exp_f32 v64, v64\n\t" \
  "v_exp_f32 v65, v65\n\t" \
  "v_smfmac_f32_32x32x32_bf16 v[24:39], v[114:117], v[52:59], v136\n\t" \
  "v_exp_f32 v66, v66\n\t" \
  "v_exp_f32 v67, v67\n\t" \
  "v_exp_f32 v68, v68\n\t" \
  "v_exp_f32 v69, v69\n\t" \
  "v_smfmac_f32_32x32x32_bf16 v[24:39], v[118:121], v[60:67], v136\n\t" \
  "v_exp_f32 v70, v70\n\t" \
  "v_exp_f32 v71, v71\n\t" \
  "v_exp_f32 v72, v72\n\t" \
  "v_exp_f32 v73, v73\n\t" \
  "s_waitcnt vmcnt(2)\n\t" \
  "s_mov_b64 s[50:51], exec\n\t" \
  "s_mov_b32 exec_lo, %[pm]\n\t" \
  "s_mov_b32 exec_hi, 0\n\t" \
  "s_cbranch_execz LA_np_%=\n\t" \
  "v_mov_b32 v138, s52\n\t" \
  "v_mov_b32 v139, s53\n\t" \
  "global_store_dwordx2 v244, v[138:139], %[wsb] sc1\n\t" \
  "LA_np_%=:\n\t" \
  "s_mov_b64 exec, s[50:51]\n\t" \
  "v_mfma_f32_32x32x16_bf16 v[52:67], v[122:125], v[84:87], 0\n\t" \
  "v_exp_f32 v74, v74\n\t" \
  "v_exp_f32 v75, v75\n\t" \
  "v_exp_f32 v76, v76\n\t" \
  "v_exp_f32 v77, v77\n\t" \
  "v_mfma_f32_32x32x8_bf16 v[52:67], v[126:127], v[88:89], v[52:67]\n\t" \
  "buffer_load_dwordx4 v[108:111], v236, %[rs], s40 offen sc1\n\t" \
  "buffer_load_dwordx2 v[112:113], v237, %[rs], s40 offen offset:3072 sc1\n\t" \
  "v_exp_f32 v78, v78\n\t" \
  "v_exp_f32 v79, v79\n\t" \
  "v_exp_f32 v80, v80\n\t" \
  "v_exp_f32 v81, v81\n\t" \
  "v_smfmac_f32_32x32x32_bf16 v[36:51], v[114:117], v[68:75], v136\n\t" \
  "v_exp_f32 v82, v82\n\t" \
  "v_exp_f32 v83, v83\n\t" \
  "v_exp_f32 v52, v52\n\t" \
  "v_exp_f32 v53, v53\n\t" \
  "v_smfmac_f32_32x32x32_bf16 v[36:51], v[118:121], v[76:83], v136\n\t" \
  "v_exp_f32 v54, v54\n\t" \
  "v_exp_f32 v55, v55\n\t" \
  "v_exp_f32 v56, v56\n\t" \
  "v_exp_f32 v57, v57\n\t" \
  "v_mfma_f32_32x32x16_bf16 v[68:83], v[122:125], v[90:93], 0\n\t" \
  "v_exp_f32 v58, v58\n\t" \
  "v_exp_f32 v59, v59\n\t" \
  "v_exp_f32 v60, v60\n\t" \
  "v_exp_f32 v61, v61\n\t" \
  "v_mfma_f32_32x32x8_bf16 v[68:83], v[126:127], v[94:95], v[68:83]\n\t" \
  "v_exp_f32 v62, v62\n\t" \
  "v_exp_f32 v63, v63\n\t" \
  "v_exp_f32 v64, v64\n\t" \
  "v_exp_f32 v65, v65\n\t" \
  "s_waitcnt vmcnt(2)\n\t" \
  "v_smfmac_f32_32x32x32_bf16 v[0:15], v[128:131], v[52:59], v136\n\t" \
  "buffer_load_dwordx4 v[114:117], v238, %[rs], s40 offen offset:1024 sc1\n\t" \
  "buffer_load_dwordx4 v[118:121], v238, %[rs], s40 offen offset:2048 sc1\n\t" \
  "s_add_u32 s40, s40, 3584\n\t" \
  "s_cmp_eq_u32 s40, s41\n\t" \
  "s_cselect_b32 s40, %[kb], s40\n\t" \
  "v_exp_f32 v66, v66\n\t" \
  "v_exp_f32 v67, v67\n\t" \
  "v_exp_f32 v68, v68\n\t" \
  "v_exp_f32 v69, v69\n\t" \
  "v_smfmac_f32_32x32x32_bf16 v[0:15], v[132:135], v[60:67], v136\n\t" \
  "v_exp_f32 v70, v70\n\t" \
  "v_exp_f32 v71, v71\n\t" \
  "v_exp_f32 v72, v72\n\t" \
  "v_exp_f32 v73, v73\n\t" \
  "v_mfma_f32_32x32x16_bf16 v[52:67], v[122:125], v[96:99], 0\n\t" \
  "v_exp_f32 v74, v74\n\t" \
  "v_exp_f32 v75, v75\n\t" \
  "v_exp_f32 v76, v76\n\t" \
  "v_exp_f32 v77, v77\n\t" \
  "v_mfma_f32_32x32x8_bf16 v[52:67], v[126:127], v[100:101], v[52:67]\n\t" \
  "global_load_dword v240, v240, %[wsb] sc1\n\t" \
  "global_load_dword v242, v241, %[wsb] sc1\n\t" \
  "global_load_dword v243, v241, %[wsb] offset:64 sc1\n\t" \
  "v_exp_f32 v78, v78\n\t" \
  "v_exp_f32 v79, v79\n\t" \
  "v_exp_f32 v80, v80\n\t" \
  "v_exp_f32 v81, v81\n\t" \
  "v_smfmac_f32_32x32x32_bf16 v[12:27], v[128:131], v[68:75], v136\n\t" \
  "v_exp_f32 v82, v82\n\t" \
  "v_exp_f32 v83, v83\n\t" \
  "v_exp_f32 v52, v52\n\t" \
  "v_exp_f32 v53, v53\n\t" \
  "v_smfmac_f32_32x32x32_bf16 v[12:27], v[132:135], v[76:83], v136\n\t" \
  "v_exp_f32 v54, v54\n\t" \
  "v_exp_f32 v55, v55\n\t" \
  "v_exp_f32 v56, v56\n\t" \
  "v_exp_f32 v57, v57\n\t" \
  "v_mfma_f32_32x32x16_bf16 v[68:83], v[122:125], v[102:105], 0\n\t" \
  "v_exp_f32 v58, v58\n\t" \
  "v_exp_f32 v59, v59\n\t" \
  "v_exp_f32 v60, v60\n\t" \
  "v_exp_f32 v61, v61\n\t" \
  "v_mfma_f32_32x32x8_bf16 v[68:83], v[126:127], v[106:107], v[68:83]\n\t" \
  "v_exp_f32 v62, v62\n\t" \
  "v_exp_f32 v63, v63\n\t" \
  "v_exp_f32 v64, v64\n\t" \
  "v_exp_f32 v65, v65\n\t" \
  "v_smfmac_f32_32x32x32_bf16 v[24:39], v[128:131], v[52:59], v136\n\t" \
  "v_exp_f32 v66, v66\n\t" \
  "v_exp_f32 v67, v67\n\t" \
  "v_exp_f32 v68, v68\n\t" \
  "v_exp_f32 v69, v69\n\t" \
  "v_smfmac_f32_32x32x32_bf16 v[24:39], v[132:135], v[60:67], v136\n\t" \
  "v_exp_f32 v70, v70\n\t" \
  "v_exp_f32 v71, v71\n\t" \
  "v_exp_f32 v72, v72\n\t" \
  "v_exp_f32 v73, v73\n\t" \
  "s_waitcnt vmcnt(5)\n\t" \
  "v_mfma_f32_32x32x16_bf16 v[52:67], v[108:111], v[84:87], 0\n\t" \
  "v_exp_f32 v74, v74\n\t" \
  "v_exp_f32 v75, v75\n\t" \
  "v_exp_f32 v76, v76\n\t" \
  "v_exp_f32 v77, v77\n\t" \
  "v_mfma_f32_32x32x8_bf16 v[52:67], v[112:113], v[88:89], v[52:67]\n\t" \
  "buffer_load_dwordx4 v[122:125], v236, %[rs], s40 offen sc1\n\t" \
  "buffer_load_dwordx2 v[126:127], v237, %[rs], s40 offen offset:3072 sc1\n\t" \
  "v_exp_f32 v78, v78\n\t" \
  "v_exp_f32 v79, v79\n\t" \
  "v_exp_f32 v80, v80\n\t" \
  "v_exp_f32 v81, v81\n\t" \
  "v_smfmac_f32_32x32x32_bf16 v[36:51], v[128:131], v[68:75], v136\n\t" \
  "v_exp_f32 v82, v82\n\t" \
  "v_exp_f32 v83, v83\n\t" \
  "v_exp_f32 v52, v52\n\t" \
  "v_exp_f32 v53, v53\n\t" \
  "v_smfmac_f32_32x32x32_bf16 v[36:51], v[132:135], v[76:83], v136\n\t" \
  "v_exp_f32 v54, v54\n\t" \
  "v_exp_f32 v55, v55\n\t" \
  "v_exp_f32 v56, v56\n\t" \
  "v_exp_f32 v57, v57\n\t" \
  "v_mfma_f32_32x32x16_bf16 v[68:83], v[108:111], v[90:93], 0\n\t" \
  "v_exp_f32 v58, v58\n\t" \
  "v_exp_f32 v59, v59\n\t" \
  "v_exp_f32 v60, v60\n\t" \
  "v_exp_f32 v61, v61\n\t" \
  "v_mfma_f32_32x32x8_bf16 v[68:83], v[112:113], v[94:95], v[68:83]\n\t" \
  "v_exp_f32 v62, v62\n\t" \
  "v_exp_f32 v63, v63\n\t" \
  "v_exp_f32 v64, v64\n\t" \
  "v_exp_f32 v65, v65\n\t" \
  "s_waitcnt vmcnt(5)\n\t" \
  "v_smfmac_f32_32x32x32_bf16 v[0:15], v[114:117], v[52:59], v136\n\t" \
  "buffer_load_dwordx4 v[128:131], v238, %[rs], s40 offen offset:1024 sc1\n\t" \
  "buffer_load_dwordx4 v[132:135], v238, %[rs], s40 offen offset:2048 sc1\n\t" \
  "s_add_u32 s40, s40, 3584\n\t" \
  "s_cmp_eq_u32 s40, s41\n\t" \
  "s_cselect_b32 s40, %[kb], s40\n\t" \
  "v_exp_f32 v66, v66\n\t" \
  "v_exp_f32 v67, v67\n\t" \
  "v_exp_f32 v68, v68\n\t" \
  "v_exp_f32 v69, v69\n\t" \
  "v_smfmac_f32_32x32x32_bf16 v[0:15], v[118:121], v[60:67], v136\n\t" \
  "v_exp_f32 v70, v70\n\t" \
  "v_exp_f32 v71, v71\n\t" \
  "v_exp_f32 v72, v72\n\t" \
  "v_exp_f32 v73, v73\n\t" \
  "v_mfma_f32_32x32x16_bf16 v[52:67], v[108:111], v[96:99], 0\n\t" \
  "v_exp_f32 v74, v74\n\t" \
  "v_exp_f32 v75, v75\n\t" \
  "v_exp_f32 v76, v76\n\t" \
  "v_exp_f32 v77, v77\n\t" \
  "v_mfma_f32_32x32x8_bf16 v[52:67], v[112:113], v[100:101], v[52:67]\n\t" \
  "v_exp_f32 v78, v78\n\t" \
  "v_exp_f32 v79, v79\n\t" \
  "v_exp_f32 v80, v80\n\t" \
  "v_exp_f32 v81, v81\n\t" \
  "v_smfmac_f32_32x32x32_bf16 v[12:27], v[114:117], v[68:75], v136\n\t" \
  "v_exp_f32 v82, v82\n\t" \
  "v_exp_f32 v83, v83\n\t" \
  "v_exp_f32 v52, v52\n\t" \
  "v_exp_f32 v53, v53\n\t" \
  "v_smfmac_f32_32x32x32_bf16 v[12:27], v[118:121], v[76:83], v136\n\t" \
  "v_exp_f32 v54, v54\n\t" \
  "v_exp_f32 v55, v55\n\t" \
  "v_exp_f32 v56, v56\n\t" \
  "v_exp_f32 v57, v57\n\t" \
  "v_mfma_f32_32x32x16_bf16 v[68:83], v[108:111], v[102:105], 0\n\t" \
  "v_exp_f32 v58, v58\n\t" \
  "v_exp_f32 v59, v59\n\t" \
  "v_exp_f32 v60, v60\n\t" \
  "v_exp_f32 v61, v61\n\t" \
  "v_mfma_f32_32x32x8_bf16 v[68:83], v[112:113], v[106:107], v[68:83]\n\t" \
  "v_exp_f32 v62, v62\n\t" \
  "v_exp_f32 v63, v63\n\t" \
  "v_exp_f32 v64, v64\n\t" \
  "v_exp_f32 v65, v65\n\t" \
  "v_smfmac_f32_32x32x32_bf16 v[24:39], v[114:117], v[52:59], v136\n\t" \
  "v_exp_f32 v66, v66\n\t" \
  "v_exp_f32 v67, v67\n\t" \
  "v_exp_f32 v68, v68\n\t" \
  "v_exp_f32 v69, v69\n\t" \
  "v_smfmac_f32_32x32x32_bf16 v[24:39], v[118:121], v[60:67], v136\n\t" \
  "v_exp_f32 v70, v70\n\t" \
  "v_exp_f32 v71, v71\n\t" \
  "v_exp_f32 v72, v72\n\t" \
  "v_exp_f32 v73, v73\n\t" \
  "s_waitcnt vmcnt(2)\n\t" \
  "v_mfma_f32_32x32x16_bf16 v[52:67], v[122:125], v[84:87], 0\n\t" \
  "v_exp_f32 v74, v74\n\t" \
  "v_exp_f32 v75, v75\n\t" \
  "v_exp_f32 v76, v76\n\t" \
  "v_exp_f32 v77, v77\n\t" \
  "v_mfma_f32_32x32x8_bf16 v[52:67], v[126:127], v[88:89], v[52:67]\n\t" \
  "buffer_load_dwordx4 v[108:111], v236, %[rs], s40 offen sc1\n\t" \
  "buffer_load_dwordx2 v[112:113], v237, %[rs], s40 offen offset:3072 sc1\n\t" \
  "v_exp_f32 v78, v78\n\t" \
  "v_exp_f32 v79, v79\n\t" \
  "v_exp_f32 v80, v80\n\t" \
  "v_exp_f32 v81, v81\n\t" \
  "v_smfmac_f32_32x32x32_bf16 v[36:51], v[114:117], v[68:75], v136\n\t" \
  "v_exp_f32 v82, v82\n\t" \
  "v_exp_f32 v83, v83\n\t" \
  "v_exp_f32 v52, v52\n\t" \
  "v_exp_f32 v53, v53\n\t" \
  "v_smfmac_f32_32x32x32_bf16 v[36:51], v[118:121], v[76:83], v136\n\t" \
  "v_exp_f32 v54, v54\n\t" \
  "v_exp_f32 v55, v55\n\t" \
  "v_exp_f32 v56, v56\n\t" \
  "v_exp_f32 v57, v57\n\t" \
  "v_mfma_f32_32x32x16_bf16 v[68:83], v[122:125], v[90:93], 0\n\t" \
  "v_exp_f32 v58, v58\n\t" \
  "v_exp_f32 v59, v59\n\t" \
  "v_exp_f32 v60, v60\n\t" \
  "v_exp_f32 v61, v61\n\t" \
  "v_mfma_f32_32x32x8_bf16 v[68:83], v[126:127], v[94:95], v[68:83]\n\t" \
  "v_exp_f32 v62, v62\n\t" \
  "v_exp_f32 v63, v63\n\t" \
  "v_exp_f32 v64, v64\n\t" \
  "v_exp_f32 v65, v65\n\t" \
  "s_waitcnt vmcnt(2)\n\t" \
  "v_smfmac_f32_32x32x32_bf16 v[0:15], v[128:131], v[52:59], v136\n\t" \
  "buffer_load_dwordx4 v[114:117], v238, %[rs], s40 offen offset:1024 sc1\n\t" \
  "buffer_load_dwordx4 v[118:121], v238, %[rs], s40 offen offset:2048 sc1\n\t" \
  "s_add_u32 s40, s40, 3584\n\t" \
  "s_cmp_eq_u32 s40, s41\n\t" \
  "s_cselect_b32 s40, %[kb], s40\n\t" \
  "v_exp_f32 v66, v66\n\t" \
  "v_exp_f32 v67, v67\n\t" \
  "v_exp_f32 v68, v68\n\t" \
  "v_exp_f32 v69, v69\n\t" \
  "v_smfmac_f32_32x32x32_bf16 v[0:15], v[132:135], v[60:67], v136\n\t" \
  "v_exp_f32 v70, v70\n\t" \
  "v_exp_f32 v71, v71\n\t" \
  "v_exp_f32 v72, v72\n\t" \
  "v_exp_f32 v73, v73\n\t" \
  "v_mfma_f32_32x32x16_bf16 v[52:67], v[122:125], v[96:99], 0\n\t" \
  "v_exp_f32 v74, v74\n\t" \
  "v_exp_f32 v75, v75\n\t" \
  "v_exp_f32 v76, v76\n\t" \
  "v_exp_f32 v77, v77\n\t" \
  "v_mfma_f32_32x32x8_bf16 v[52:67], v[126:127], v[100:101], v[52:67]\n\t" \
  "v_exp_f32 v78, v78\n\t" \
  "v_exp_f32 v79, v79\n\t" \
  "v_exp_f32 v80, v80\n\t" \
  "v_exp_f32 v81, v81\n\t" \
  "v_smfmac_f32_32x32x32_bf16 v[12:27], v[128:131], v[68:75], v136\n\t" \
  "v_exp_f32 v82, v82\n\t" \
  "v_exp_f32 v83, v83\n\t" \
  "v_exp_f32 v52, v52\n\t" \
  "v_exp_f32 v53, v53\n\t" \
  "v_smfmac_f32_32x32x32_bf16 v[12:27], v[132:135], v[76:83], v136\n\t" \
  "v_exp_f32 v54, v54\n\t" \
  "v_exp_f32 v55, v55\n\t" \
  "v_exp_f32 v56, v56\n\t" \
  "v_exp_f32 v57, v57\n\t" \
  "v_mfma_f32_32x32x16_bf16 v[68:83], v[122:125], v[102:105], 0\n\t" \
  "v_exp_f32 v58, v58\n\t" \
  "v_exp_f32 v59, v59\n\t" \
  "v_exp_f32 v60, v60\n\t" \
  "v_exp_f32 v61, v61\n\t" \
  "v_mfma_f32_32x32x8_bf16 v[68:83], v[126:127], v[106:107], v[68:83]\n\t" \
  "v_exp_f32 v62, v62\n\t" \
  "v_exp_f32 v63, v63\n\t" \
  "v_exp_f32 v64, v64\n\t" \
  "v_exp_f32 v65, v65\n\t" \
  "v_smfmac_f32_32x32x32_bf16 v[24:39], v[128:131], v[52:59], v136\n\t" \
  "v_exp_f32 v66, v66\n\t" \
  "v_exp_f32 v67, v67\n\t" \
  "v_exp_f32 v68, v68\n\t" \
  "v_exp_f32 v69, v69\n\t" \
  "v_smfmac_f32_32x32x32_bf16 v[24:39], v[132:135], v[60:67], v136\n\t" \
  "v_exp_f32 v70, v70\n\t" \
  "v_exp_f32 v71, v71\n\t" \
  "v_exp_f32 v72, v72\n\t" \
  "v_exp_f32 v73, v73\n\t" \
  "s_waitcnt vmcnt(2)\n\t" \
  "v_mfma_f32_32x32x16_bf16 v[52:67], v[108:111], v[84:87], 0\n\t" \
  "v_exp_f32 v74, v74\n\t" \
  "v_exp_f32 v75, v75\n\t" \
  "v_exp_f32 v76, v76\n\t" \
  "v_exp_f32 v77, v77\n\t" \
  "v_mfma_f32_32x32x8_bf16 v[52:67], v[112:113], v[88:89], v[52:67]\n\t" \
  "buffer_load_dwordx4 v[122:125], v236, %[rs], s40 offen sc1\n\t" \
  "buffer_load_dwordx2 v[126:127], v237, %[rs], s40 offen offset:3072 sc1\n\t" \
  "v_exp_f32 v78, v78\n\t" \
  "v_exp_f32 v79, v79\n\t" \
  "v_exp_f32 v80, v80\n\t" \
  "v_exp_f32 v81, v81\n\t" \
  "v_smfmac_f32_32x32x32_bf16 v[36:51], v[128:131], v[68:75], v136\n\t" \
  "v_exp_f32 v82, v82\n\t" \
  "v_exp_f32 v83, v83\n\t" \
  "v_exp_f32 v52, v52\n\t" \
  "v_exp_f32 v53, v53\n\t" \
  "v_smfmac_f32_32x32x32_bf16 v[36:51], v[132:135], v[76:83], v136\n\t" \
  "v_exp_f32 v54, v54\n\t" \
  "v_exp_f32 v55, v55\n\t" \
  "v_exp_f32 v56, v56\n\t" \
  "v_exp_f32 v57, v57\n\t" \
  "v_mfma_f32_32x32x16_bf16 v[68:83], v[108:111], v[90:93], 0\n\t" \
  "v_exp_f32 v58, v58\n\t" \
  "v_exp_f32 v59, v59\n\t" \
  "v_exp_f32 v60, v60\n\t" \
  "v_exp_f32 v61, v61\n\t" \
  "v_mfma_f32_32x32x8_bf16 v[68:83], v[112:113], v[94:95], v[68:83]\n\t" \
  "v_exp_f32 v62, v62\n\t" \
  "v_exp_f32 v63, v63\n\t" \
  "v_exp_f32 v64, v64\n\t" \
  "v_exp_f32 v65, v65\n\t" \
  "s_waitcnt vmcnt(2)\n\t" \
  "v_smfmac_f32_32x32x32_bf16 v[0:15], v[114:117], v[52:59], v136\n\t" \
  "buffer_load_dwordx4 v[128:131], v238, %[rs], s40 offen offset:1024 sc1\n\t" \
  "buffer_load_dwordx4 v[132:135], v238, %[rs], s40 offen offset:2048 sc1\n\t" \
  "s_add_u32 s40, s40, 3584\n\t" \
  "s_cmp_eq_u32 s40, s41\n\t" \
  "s_cselect_b32 s40, %[kb], s40\n\t" \
  "v_exp_f32 v66, v66\n\t" \
  "v_exp_f32 v67, v67\n\t" \
  "v_exp_f32 v68, v68\n\t" \
  "v_exp_f32 v69, v69\n\t" \
  "v_smfmac_f32_32x32x32_bf16 v[0:15], v[118:121], v[60:67], v136\n\t" \
  "v_exp_f32 v70, v70\n\t" \
  "v_exp_f32 v71, v71\n\t" \
  "v_exp_f32 v72, v72\n\t" \
  "v_exp_f32 v73, v73\n\t" \
  "v_mfma_f32_32x32x16_bf16 v[52:67], v[108:111], v[96:99], 0\n\t" \
  "v_exp_f32 v74, v74\n\t" \
  "v_exp_f32 v75, v75\n\t" \
  "v_exp_f32 v76, v76\n\t" \
  "v_exp_f32 v77, v77\n\t" \
  "v_mfma_f32_32x32x8_bf16 v[52:67], v[112:113], v[100:101], v[52:67]\n\t" \
  "v_exp_f32 v78, v78\n\t" \
  "v_exp_f32 v79, v79\n\t" \
  "v_exp_f32 v80, v80\n\t" \
  "v_exp_f32 v81, v81\n\t" \
  "v_smfmac_f32_32x32x32_bf16 v[12:27], v[114:117], v[68:75], v136\n\t" \
  "v_exp_f32 v82, v82\n\t" \
  "v_exp_f32 v83, v83\n\t" \
  "v_exp_f32 v52, v52\n\t" \
  "v_exp_f32 v53, v53\n\t" \
  "v_smfmac_f32_32x32x32_bf16 v[12:27], v[118:121], v[76:83], v136\n\t" \
  "v_exp_f32 v54, v54\n\t" \
  "v_exp_f32 v55, v55\n\t" \
  "v_exp_f32 v56, v56\n\t" \
  "v_exp_f32 v57, v57\n\t" \
  "v_mfma_f32_32x32x16_bf16 v[68:83], v[108:111], v[102:105], 0\n\t" \
  "v_exp_f32 v58, v58\n\t" \
  "v_exp_f32 v59, v59\n\t" \
  "v_exp_f32 v60, v60\n\t" \
  "v_exp_f32 v61, v61\n\t" \
  "v_mfma_f32_32x32x8_bf16 v[68:83], v[112:113], v[106:107], v[68:83]\n\t" \
  "v_exp_f32 v62, v62\n\t" \
  "v_exp_f32 v63, v63\n\t" \
  "v_exp_f32 v64, v64\n\t" \
  "v_exp_f32 v65, v65\n\t" \
  "v_smfmac_f32_32x32x32_bf16 v[24:39], v[114:117], v[52:59], v136\n\t" \
  "v_exp_f32 v66, v66\n\t" \
  "v_exp_f32 v67, v67\n\t" \
  "v_exp_f32 v68, v68\n\t" \
  "v_exp_f32 v69, v69\n\t" \
  "v_smfmac_f32_32x32x32_bf16 v[24:39], v[118:121], v[60:67], v136\n\t" \
  "v_exp_f32 v70, v70\n\t" \
  "v_exp_f32 v71, v71\n\t" \
  "v_exp_f32 v72, v72\n\t" \
  "v_exp_f32 v73, v73\n\t" \
  "s_waitcnt vmcnt(2)\n\t" \
  "v_mfma_f32_32x32x16_bf16 v[52:67], v[122:125], v[84:87], 0\n\t" \
  "v_exp_f32 v74, v74\n\t" \
  "v_exp_f32 v75, v75\n\t" \
  "v_exp_f32 v76, v76\n\t" \
  "v_exp_f32 v77, v77\n\t" \
  "v_mfma_f32_32x32x8_bf16 v[52:67], v[126:127], v[88:89], v[52:67]\n\t" \
  "buffer_load_dwordx4 v[108:111], v236, %[rs], s40 offen sc1\n\t" \
  "buffer_load_dwordx2 v[112:113], v237, %[rs], s40 offen offset:3072 sc1\n\t" \
  "v_exp_f32 v78, v78\n\t" \
  "v_exp_f32 v79, v79\n\t" \
  "v_exp_f32 v80, v80\n\t" \
  "v_exp_f32 v81, v81\n\t" \
  "v_smfmac_f32_32x32x32_bf16 v[36:51], v[114:117], v[68:75], v136\n\t" \
  "v_exp_f32 v82, v82\n\t" \
  "v_exp_f32 v83, v83\n\t" \
  "v_exp_f32 v52, v52\n\t" \
  "v_exp_f32 v53, v53\n\t" \
  "v_smfmac_f32_32x32x32_bf16 v[36:51], v[118:121], v[76:83], v136\n\t" \
  "v_exp_f32 v54, v54\n\t" \
  "v_exp_f32 v55, v55\n\t" \
  "v_exp_f32 v56, v56\n\t" \
  "v_exp_f32 v57, v57\n\t" \
  "v_mfma_f32_32x32x16_bf16 v[68:83], v[122:125], v[90:93], 0\n\t" \
  "v_exp_f32 v58, v58\n\t" \
  "v_exp_f32 v59, v59\n\t" \
  "v_exp_f32 v60, v60\n\t" \
  "v_exp_f32 v61, v61\n\t" \
  "v_mfma_f32_32x32x8_bf16 v[68:83], v[126:127], v[94:95], v[68:83]\n\t" \
  "v_exp_f32 v62, v62\n\t" \
  "v_exp_f32 v63, v63\n\t" \
  "v_exp_f32 v64, v64\n\t" \
  "v_exp_f32 v65, v65\n\t" \
  "s_waitcnt vmcnt(2)\n\t" \
  "v_smfmac_f32_32x32x32_bf16 v[0:15], v[128:131], v[52:59], v136\n\t" \
  "buffer_load_dwordx4 v[114:117], v238, %[rs], s40 offen offset:1024 sc1\n\t" \
  "buffer_load_dwordx4 v[118:121], v238, %[rs], s40 offen offset:2048 sc1\n\t" \
  "s_add_u32 s40, s40, 3584\n\t" \
  "s_cmp_eq_u32 s40, s41\n\t" \
  "s_cselect_b32 s40, %[kb], s40\n\t" \
  "v_exp_f32 v66, v66\n\t" \
  "v_exp_f32 v67, v67\n\t" \
  "v_exp_f32 v68, v68\n\t" \
  "v_exp_f32 v69, v69\n\t" \
  "v_smfmac_f32_32x32x32_bf16 v[0:15], v[132:135], v[60:67], v136\n\t" \
  "v_exp_f32 v70, v70\n\t" \
  "v_exp_f32 v71, v71\n\t" \
  "v_exp_f32 v72, v72\n\t" \
  "v_exp_f32 v73, v73\n\t" \
  "v_mfma_f32_32x32x16_bf16 v[52:67], v[122:125], v[96:99], 0\n\t" \
  "v_exp_f32 v74, v74\n\t" \
  "v_exp_f32 v75, v75\n\t" \
  "v_exp_f32 v76, v76\n\t" \
  "v_exp_f32 v77, v77\n\t" \
  "v_mfma_f32_32x32x8_bf16 v[52:67], v[126:127], v[100:101], v[52:67]\n\t" \
  "v_exp_f32 v78, v78\n\t" \
  "v_exp_f32 v79, v79\n\t" \
  "v_exp_f32 v80, v80\n\t" \
  "v_exp_f32 v81, v81\n\t" \
  "v_smfmac_f32_32x32x32_bf16 v[12:27], v[128:131], v[68:75], v136\n\t" \
  "v_exp_f32 v82, v82\n\t" \
  "v_exp_f32 v83, v83\n\t" \
  "v_exp_f32 v52, v52\n\t" \
  "v_exp_f32 v53, v53\n\t" \
  "v_smfmac_f32_32x32x32_bf16 v[12:27], v[132:135], v[76:83], v136\n\t" \
  "v_exp_f32 v54, v54\n\t" \
  "v_exp_f32 v55, v55\n\t" \
  "v_exp_f32 v56, v56\n\t" \
  "v_exp_f32 v57, v57\n\t" \
  "v_mfma_f32_32x32x16_bf16 v[68:83], v[122:125], v[102:105], 0\n\t" \
  "v_exp_f32 v58, v58\n\t" \
  "v_exp_f32 v59, v59\n\t" \
  "v_exp_f32 v60, v60\n\t" \
  "v_exp_f32 v61, v61\n\t" \
  "v_mfma_f32_32x32x8_bf16 v[68:83], v[126:127], v[106:107], v[68:83]\n\t" \
  "v_exp_f32 v62, v62\n\t" \
  "v_exp_f32 v63, v63\n\t" \
  "v_exp_f32 v64, v64\n\t" \
  "v_exp_f32 v65, v65\n\t" \
  "v_smfmac_f32_32x32x32_bf16 v[24:39], v[128:131], v[52:59], v136\n\t" \
  "v_exp_f32 v66, v66\n\t" \
  "v_exp_f32 v67, v67\n\t" \
  "v_exp_f32 v68, v68\n\t" \
  "v_exp_f32 v69, v69\n\t" \
  "v_smfmac_f32_32x32x32_bf16 v[24:39], v[132:135], v[60:67], v136\n\t" \
  "v_exp_f32 v70, v70\n\t" \
  "v_exp_f32 v71, v71\n\t" \
  "v_exp_f32 v72, v72\n\t" \
  "v_exp_f32 v73, v73\n\t" \
  "s_waitcnt vmcnt(2)\n\t" \
  "v_mfma_f32_32x32x16_bf16 v[52:67], v[108:111], v[84:87], 0\n\t" \
  "v_exp_f32 v74, v74\n\t" \
  "v_exp_f32 v75, v75\n\t" \
  "v_exp_f32 v76, v76\n\t" \
  "v_exp_f32 v77, v77\n\t" \
  "v_mfma_f32_32x32x8_bf16 v[52:67], v[112:113], v[88:89], v[52:67]\n\t" \
  "buffer_load_dwordx4 v[122:125], v236, %[rs], s40 offen sc1\n\t" \
  "buffer_load_dwordx2 v[126:127], v237, %[rs], s40 offen offset:3072 sc1\n\t" \
  "v_exp_f32 v78, v78\n\t" \
  "v_exp_f32 v79, v79\n\t" \
  "v_exp_f32 v80, v80\n\t" \
  "v_exp_f32 v81, v81\n\t" \
  "v_smfmac_f32_32x32x32_bf16 v[36:51], v[128:131], v[68:75], v136\n\t" \
  "v_exp_f32 v82, v82\n\t" \
  "v_exp_f32 v83, v83\n\t" \
  "v_exp_f32 v52, v52\n\t" \
  "v_exp_f32 v53, v53\n\t" \
  "v_smfmac_f32_32x32x32_bf16 v[36:51], v[132:135], v[76:83], v136\n\t" \
  "v_exp_f32 v54, v54\n\t" \
  "v_exp_f32 v55, v55\n\t" \
  "v_exp_f32 v56, v56\n\t" \
  "v_exp_f32 v57, v57\n\t" \
  "v_mfma_f32_32x32x16_bf16 v[68:83], v[108:111], v[90:93], 0\n\t" \
  "v_exp_f32 v58, v58\n\t" \
  "v_exp_f32 v59, v59\n\t" \
  "v_exp_f32 v60, v60\n\t" \
  "v_exp_f32 v61, v61\n\t" \
  "v_mfma_f32_32x32x8_bf16 v[68:83], v[112:113], v[94:95], v[68:83]\n\t" \
  "v_exp_f32 v62, v62\n\t" \
  "v_exp_f32 v63, v63\n\t" \
  "v_exp_f32 v64, v64\n\t" \
  "v_exp_f32 v65, v65\n\t" \
  "s_waitcnt vmcnt(2)\n\t" \
  "v_smfmac_f32_32x32x32_bf16 v[0:15], v[114:117], v[52:59], v136\n\t" \
  "buffer_load_dwordx4 v[128:131], v238, %[rs], s40 offen offset:1024 sc1\n\t" \
  "buffer_load_dwordx4 v[132:135], v238, %[rs], s40 offen offset:2048 sc1\n\t" \
  "s_add_u32 s40, s40, 3584\n\t" \
  "s_cmp_eq_u32 s40, s41\n\t" \
  "s_cselect_b32 s40, %[kb], s40\n\t" \
  "v_exp_f32 v66, v66\n\t" \
  "v_exp_f32 v67, v67\n\t" \
  "v_exp_f32 v68, v68\n\t" \
  "v_exp_f32 v69, v69\n\t" \
  "v_smfmac_f32_32x32x32_bf16 v[0:15], v[118:121], v[60:67], v136\n\t" \
  "v_exp_f32 v70, v70\n\t" \
  "v_exp_f32 v71, v71\n\t" \
  "v_exp_f32 v72, v72\n\t" \
  "v_exp_f32 v73, v73\n\t" \
  "v_mfma_f32_32x32x16_bf16 v[52:67], v[108:111], v[96:99], 0\n\t" \
  "v_exp_f32 v74, v74\n\t" \
  "v_exp_f32 v75, v75\n\t" \
  "v_exp_f32 v76, v76\n\t" \
  "v_exp_f32 v77, v77\n\t" \
  "v_mfma_f32_32x32x8_bf16 v[52:67], v[112:113], v[100:101], v[52:67]\n\t" \
  "v_exp_f32 v78, v78\n\t" \
  "v_exp_f32 v79, v79\n\t" \
  "v_exp_f32 v80, v80\n\t" \
  "v_exp_f32 v81, v81\n\t" \
  "v_smfmac_f32_32x32x32_bf16 v[12:27], v[114:117], v[68:75], v136\n\t" \
  "v_exp_f32 v82, v82\n\t" \
  "v_exp_f32 v83, v83\n\t" \
  "v_exp_f32 v52, v52\n\t" \
  "v_exp_f32 v53, v53\n\t" \
  "v_smfmac_f32_32x32x32_bf16 v[12:27], v[118:121], v[76:83], v136\n\t" \
  "v_exp_f32 v54, v54\n\t" \
  "v_exp_f32 v55, v55\n\t" \
  "v_exp_f32 v56, v56\n\t" \
  "v_exp_f32 v57, v57\n\t" \
  "v_mfma_f32_32x32x16_bf16 v[68:83], v[108:111], v[102:105], 0\n\t" \
  "v_exp_f32 v58, v58\n\t" \
  "v_exp_f32 v59, v59\n\t" \
  "v_exp_f32 v60, v60\n\t" \
  "v_exp_f32 v61, v61\n\t" \
  "v_mfma_f32_32x32x8_bf16 v[68:83], v[112:113], v[106:107], v[68:83]\n\t" \
  "v_exp_f32 v62, v62\n\t" \
  "v_exp_f32 v63, v63\n\t" \
  "v_exp_f32 v64, v64\n\t" \
  "v_exp_f32 v65, v65\n\t" \
  "v_smfmac_f32_32x32x32_bf16 v[24:39], v[114:117], v[52:59], v136\n\t" \
  "v_exp_f32 v66, v66\n\t" \
  "v_exp_f32 v67, v67\n\t" \
  "v_exp_f32 v68, v68\n\t" \
  "v_exp_f32 v69, v69\n\t" \
  "v_smfmac_f32_32x32x32_bf16 v[24:39], v[118:121], v[60:67], v136\n\t" \
  "v_exp_f32 v70, v70\n\t" \
  "v_exp_f32 v71, v71\n\t" \
  "v_exp_f32 v72, v72\n\t" \
  "v_exp_f32 v73, v73\n\t" \
  "s_waitcnt vmcnt(2)\n\t" \
  "v_mfma_f32_32x32x16_bf16 v[52:67], v[122:125], v[84:87], 0\n\t" \
  "v_exp_f32 v74, v74\n\t" \
  "v_exp_f32 v75, v75\n\t" \
  "v_exp_f32 v76, v76\n\t" \
  "v_exp_f32 v77, v77\n\t" \
  "v_mfma_f32_32x32x8_bf16 v[52:67], v[126:127], v[88:89], v[52:67]\n\t" \
  "buffer_load_dwordx4 v[108:111], v236, %[rs], s40 offen sc1\n\t" \
  "buffer_load_dwordx2 v[112:113], v237, %[rs], s40 offen offset:3072 sc1\n\t" \
  "v_exp_f32 v78, v78\n\t" \
  "v_exp_f32 v79, v79\n\t" \
  "v_exp_f32 v80, v80\n\t" \
  "v_exp_f32 v81, v81\n\t" \
  "v_smfmac_f32_32x32x32_bf16 v[36:51], v[114:117], v[68:75], v136\n\t" \
  "v_exp_f32 v82, v82\n\t" \
  "v_exp_f32 v83, v83\n\t" \
  "v_exp_f32 v52, v52\n\t" \
  "v_exp_f32 v53, v53\n\t" \
  "v_smfmac_f32_32x32x32_bf16 v[36:51], v[118:121], v[76:83], v136\n\t" \
  "v_exp_f32 v54, v54\n\t" \
  "v_exp_f32 v55, v55\n\t" \
  "v_exp_f32 v56, v56\n\t" \
  "v_exp_f32 v57, v57\n\t" \
  "v_mfma_f32_32x32x16_bf16 v[68:83], v[122:125], v[90:93], 0\n\t" \
  "v_exp_f32 v58, v58\n\t" \
  "v_exp_f32 v59, v59\n\t" \
  "v_exp_f32 v60, v60\n\t" \
  "v_exp_f32 v61, v61\n\t" \
  "v_mfma_f32_32x32x8_bf16 v[68:83], v[126:127], v[94:95], v[68:83]\n\t" \
  "global_load_dwordx2 v[138:139], v137, %[wsb] sc1\n\t" \
  "v_exp_f32 v62, v62\n\t" \
  "v_exp_f32 v63, v63\n\t" \
  "v_exp_f32 v64, v64\n\t" \
  "v_exp_f32 v65, v65\n\t" \
  "s_waitcnt vmcnt(3)\n\t" \
  "v_smfmac_f32_32x32x32_bf16 v[0:15], v[128:131], v[52:59], v136\n\t" \
  "buffer_load_dwordx4 v[114:117], v238, %[rs], s40 offen offset:1024 sc1\n\t" \
  "buffer_load_dwordx4 v[118:121], v238, %[rs], s40 offen offset:2048 sc1\n\t" \
  "s_add_u32 s40, s40, 3584\n\t" \
  "s_cmp_eq_u32 s40, s41\n\t" \
  "s_cselect_b32 s40, %[kb], s40\n\t" \
  "v_exp_f32 v66, v66\n\t" \
  "v_exp_f32 v67, v67\n\t" \
  "v_exp_f32 v68, v68\n\t" \
  "v_exp_f32 v69, v69\n\t" \
  "v_smfmac_f32_32x32x32_bf16 v[0:15], v[132:135], v[60:67], v136\n\t" \
  "v_exp_f32 v70, v70\n\t" \
  "v_exp_f32 v71, v71\n\t" \
  "v_exp_f32 v72, v72\n\t" \
  "v_exp_f32 v73, v73\n\t" \
  "v_mfma_f32_32x32x16_bf16 v[52:67], v[122:125], v[96:99], 0\n\t" \
  "v_exp_f32 v74, v74\n\t" \
  "v_exp_f32 v75, v75\n\t" \
  "v_exp_f32 v76, v76\n\t" \
  "v_exp_f32 v77, v77\n\t" \
  "v_mfma_f32_32x32x8_bf16 v[52:67], v[126:127], v[100:101], v[52:67]\n\t" \
  "v_exp_f32 v78, v78\n\t" \
  "v_exp_f32 v79, v79\n\t" \
  "v_exp_f32 v80, v80\n\t" \
  "v_exp_f32 v81, v81\n\t" \
  "v_smfmac_f32_32x32x32_bf16 v[12:27], v[128:131], v[68:75], v136\n\t" \
  "v_exp_f32 v82, v82\n\t" \
  "v_exp_f32 v83, v83\n\t" \
  "v_exp_f32 v52, v52\n\t" \
  "v_exp_f32 v53, v53\n\t" \
  "v_smfmac_f32_32x32x32_bf16 v[12:27], v[132:135], v[76:83], v136\n\t" \
  "v_exp_f32 v54, v54\n\t" \
  "v_exp_f32 v55, v55\n\t" \
  "v_exp_f32 v56, v56\n\t" \
  "v_exp_f32 v57, v57\n\t" \
  "v_mfma_f32_32x32x16_bf16 v[68:83], v[122:125], v[102:105], 0\n\t" \
  "v_exp_f32 v58, v58\n\t" \
  "v_exp_f32 v59, v59\n\t" \
  "v_exp_f32 v60, v60\n\t" \
  "v_exp_f32 v61, v61\n\t" \
  "v_mfma_f32_32x32x8_bf16 v[68:83], v[126:127], v[106:107], v[68:83]\n\t" \
  "v_exp_f32 v62, v62\n\t" \
  "v_exp_f32 v63, v63\n\t" \
  "v_exp_f32 v64, v64\n\t" \
  "v_exp_f32 v65, v65\n\t" \
  "v_smfmac_f32_32x32x32_bf16 v[24:39], v[128:131], v[52:59], v136\n\t" \
  "v_exp_f32 v66, v66\n\t" \
  "v_exp_f32 v67, v67\n\t" \
  "v_exp_f32 v68, v68\n\t" \
  "v_exp_f32 v69, v69\n\t" \
  "v_smfmac_f32_32x32x32_bf16 v[24:39], v[132:135], v[60:67], v136\n\t" \
  "v_exp_f32 v70, v70\n\t" \
  "v_exp_f32 v71, v71\n\t" \
  "v_exp_f32 v72, v72\n\t" \
  "v_exp_f32 v73, v73\n\t" \
  "s_waitcnt vmcnt(3)\n\t" \
  "v_mfma_f32_32x32x16_bf16 v[52:67], v[108:111], v[84:87], 0\n\t" \
  "v_exp_f32 v74, v74\n\t" \
  "v_exp_f32 v75, v75\n\t" \
  "v_exp_f32 v76, v76\n\t" \
  "v_exp_f32 v77, v77\n\t" \
  "v_mfma_f32_32x32x8_bf16 v[52:67], v[112:113], v[88:89], v[52:67]\n\t" \
  "buffer_load_dwordx4 v[122:125], v236, %[rs], s40 offen sc1\n\t" \
  "buffer_load_dwordx2 v[126:127], v237, %[rs], s40 offen offset:3072 sc1\n\t" \
  "v_exp_f32 v78, v78\n\t" \
  "v_exp_f32 v79, v79\n\t" \
  "v_exp_f32 v80, v80\n\t" \
  "v_exp_f32 v81, v81\n\t" \
  "v_smfmac_f32_32x32x32_bf16 v[36:51], v[128:131], v[68:75], v136\n\t" \
  "v_exp_f32 v82, v82\n\t" \
  "v_exp_f32 v83, v83\n\t" \
  "v_exp_f32 v52, v52\n\t" \
  "v_exp_f32 v53, v53\n\t" \
  "v_smfmac_f32_32x32x32_bf16 v[36:51], v[132:135], v[76:83], v136\n\t" \
  "v_exp_f32 v54, v54\n\t" \
  "v_exp_f32 v55, v55\n\t" \
  "v_exp_f32 v56, v56\n\t" \
  "v_exp_f32 v57, v57\n\t" \
  "v_mfma_f32_32x32x16_bf16 v[68:83], v[108:111], v[90:93], 0\n\t" \
  "v_exp_f32 v58, v58\n\t" \
  "v_exp_f32 v59, v59\n\t" \
  "v_exp_f32 v60, v60\n\t" \
  "v_exp_f32 v61, v61\n\t" \
  "v_mfma_f32_32x32x8_bf16 v[68:83], v[112:113], v[94:95], v[68:83]\n\t" \
  "v_exp_f32 v62, v62\n\t" \
  "v_exp_f32 v63, v63\n\t" \
  "v_exp_f32 v64, v64\n\t" \
  "v_exp_f32 v65, v65\n\t" \
  "s_waitcnt vmcnt(2)\n\t" \
  "v_smfmac_f32_32x32x32_bf16 v[0:15], v[114:117], v[52:59], v136\n\t" \
  "buffer_load_dwordx4 v[128:131], v238, %[rs], s40 offen offset:1024 sc1\n\t" \
  "buffer_load_dwordx4 v[132:135], v238, %[rs], s40 offen offset:2048 sc1\n\t" \
  "s_add_u32 s40, s40, 3584\n\t" \
  "s_cmp_eq_u32 s40, s41\n\t" \
  "s_cselect_b32 s40, %[kb], s40\n\t" \
  "v_exp_f32 v66, v66\n\t" \
  "v_exp_f32 v67, v67\n\t" \
  "v_exp_f32 v68, v68\n\t" \
  "v_exp_f32 v69, v69\n\t" \
  "v_smfmac_f32_32x32x32_bf16 v[0:15], v[118:121], v[60:67], v136\n\t" \
  "v_exp_f32 v70, v70\n\t" \
  "v_exp_f32 v71, v71\n\t" \
  "v_exp_f32 v72, v72\n\t" \
  "v_exp_f32 v73, v73\n\t" \
  "v_mfma_f32_32x32x16_bf16 v[52:67], v[108:111], v[96:99], 0\n\t" \
  "v_exp_f32 v74, v74\n\t" \
  "v_exp_f32 v75, v75\n\t" \
  "v_exp_f32 v76, v76\n\t" \
  "v_exp_f32 v77, v77\n\t" \
  "v_mfma_f32_32x32x8_bf16 v[52:67], v[112:113], v[100:101], v[52:67]\n\t" \
  "v_exp_f32 v78, v78\n\t" \
  "v_exp_f32 v79, v79\n\t" \
  "v_exp_f32 v80, v80\n\t" \
  "v_exp_f32 v81, v81\n\t" \
  "v_smfmac_f32_32x32x32_bf16 v[12:27], v[114:117], v[68:75], v136\n\t" \
  "v_exp_f32 v82, v82\n\t" \
  "v_exp_f32 v83, v83\n\t" \
  "v_exp_f32 v52, v52\n\t" \
  "v_exp_f32 v53, v53\n\t" \
  "v_smfmac_f32_32x32x32_bf16 v[12:27], v[118:121], v[76:83], v136\n\t" \
  "v_exp_f32 v54, v54\n\t" \
  "v_exp_f32 v55, v55\n\t" \
  "v_exp_f32 v56, v56\n\t" \
  "v_exp_f32 v57, v57\n\t" \
  "v_mfma_f32_32x32x16_bf16 v[68:83], v[108:111], v[102:105], 0\n\t" \
  "v_exp_f32 v58, v58\n\t" \
  "v_exp_f32 v59, v59\n\t" \
  "v_exp_f32 v60, v60\n\t" \
  "v_exp_f32 v61, v61\n\t" \
  "v_mfma_f32_32x32x8_bf16 v[68:83], v[112:113], v[106:107], v[68:83]\n\t" \
  "v_exp_f32 v62, v62\n\t" \
  "v_exp_f32 v63, v63\n\t" \
  "v_exp_f32 v64, v64\n\t" \
  "v_exp_f32 v65, v65\n\t" \
  "v_smfmac_f32_32x32x32_bf16 v[24:39], v[114:117], v[52:59], v136\n\t" \
  "v_exp_f32 v66, v66\n\t" \
  "v_exp_f32 v67, v67\n\t" \
  "v_exp_f32 v68, v68\n\t" \
  "v_exp_f32 v69, v69\n\t" \
  "v_smfmac_f32_32x32x32_bf16 v[24:39], v[118:121], v[60:67], v136\n\t" \
  "v_exp_f32 v70, v70\n\t" \
  "v_exp_f32 v71, v71\n\t" \
  "v_exp_f32 v72, v72\n\t" \
  "v_exp_f32 v73, v73\n\t" \
  "s_waitcnt vmcnt(2)\n\t" \
  "v_mfma_f32_32x32x16_bf16 v[52:67], v[122:125], v[84:87], 0\n\t" \
  "v_exp_f32 v74, v74\n\t" \
  "v_exp_f32 v75, v75\n\t" \
  "v_exp_f32 v76, v76\n\t" \
  "v_exp_f32 v77, v77\n\t" \
  "v_mfma_f32_32x32x8_bf16 v[52:67], v[126:127], v[88:89], v[52:67]\n\t" \
  "buffer_load_dwordx4 v[108:111], v236, %[rs], s40 offen sc1\n\t" \
  "buffer_load_dwordx2 v[112:113], v237, %[rs], s40 offen offset:3072 sc1\n\t" \
  "v_exp_f32 v78, v78\n\t" \
  "v_exp_f32 v79, v79\n\t" \
  "v_exp_f32 v80, v80\n\t" \
  "v_exp_f32 v81, v81\n\t" \
  "v_smfmac_f32_32x32x32_bf16 v[36:51], v[114:117], v[68:75], v136\n\t" \
  "v_exp_f32 v82, v82\n\t" \
  "v_exp_f32 v83, v83\n\t" \
  "v_exp_f32 v52, v52\n\t" \
  "v_exp_f32 v53, v53\n\t" \
  "v_smfmac_f32_32x32x32_bf16 v[36:51], v[118:121], v[76:83], v136\n\t" \
  "v_exp_f32 v54, v54\n\t" \
  "v_exp_f32 v55, v55\n\t" \
  "v_exp_f32 v56, v56\n\t" \
  "v_exp_f32 v57, v57\n\t" \
  "v_mfma_f32_32x32x16_bf16 v[68:83], v[122:125], v[90:93], 0\n\t" \
  "v_exp_f32 v58, v58\n\t" \
  "v_exp_f32 v59, v59\n\t" \
  "v_exp_f32 v60, v60\n\t" \
  "v_exp_f32 v61, v61\n\t" \
  "v_mfma_f32_32x32x8_bf16 v[68:83], v[126:127], v[94:95], v[68:83]\n\t" \
  "v_cmp_ne_u64 vcc, %[tag], v[138:139]\n\t" \
  "s_mov_b32 s45, 0x400000\n\t" \
  "s_cbranch_vccnz LA_spin_%=\n\t" \
  "LA_go_%=:\n\t" \
  "v_exp_f32 v62, v62\n\t" \
  "v_exp_f32 v63, v63\n\t" \
  "v_exp_f32 v64, v64\n\t" \
  "v_exp_f32 v65, v65\n\t" \
  "s_waitcnt vmcnt(2)\n\t" \
  "v_smfmac_f32_32x32x32_bf16 v[0:15], v[128:131], v[52:59], v136\n\t" \
  "buffer_load_dwordx4 v[114:117], v238, %[rs], s40 offen offset:1024 sc1\n\t" \
  "buffer_load_dwordx4 v[118:121], v238, %[rs], s40 offen offset:2048 sc1\n\t" \
  "s_add_u32 s40, s40, 3584\n\t" \
  "s_cmp_eq_u32 s40, s41\n\t" \
  "s_cselect_b32 s40, %[kb], s40\n\t" \
  "v_exp_f32 v66, v66\n\t" \
  "v_exp_f32 v67, v67\n\t" \
  "v_exp_f32 v68, v68\n\t" \
  "v_exp_f32 v69, v69\n\t" \
  "v_smfmac_f32_32x32x32_bf16 v[0:15], v[132:135], v[60:67], v136\n\t" \
  "v_exp_f32 v70, v70\n\t" \
  "v_exp_f32 v71, v71\n\t" \
  "v_exp_f32 v72, v72\n\t" \
  "v_exp_f32 v73, v73\n\t" \
  "v_mfma_f32_32x32x16_bf16 v[52:67], v[122:125], v[96:99], 0\n\t" \
  "v_exp_f32 v74, v74\n\t" \
  "v_exp_f32 v75, v75\n\t" \
  "v_exp_f32 v76, v76\n\t" \
  "v_exp_f32 v77, v77\n\t" \
  "v_mfma_f32_32x32x8_bf16 v[52:67], v[126:127], v[100:101], v[52:67]\n\t" \
  "s_add_u32 m0, %[lw], 73728\n\t" \
  "s_nop 0\n\t" \
  "buffer_load_dwordx4 v236, %[rs], s48 offen sc1 lds\n\t" \
  "buffer_load_dwordx4 v236, %[rs], s48 offen offset:1024 sc1 lds\n\t" \
  "s_cmp_eq_u32 %[th], 0\n\t" \
  "s_cbranch_scc0 LA_o1_%=\n\t" \
  "buffer_load_dwordx4 v236, %[rs], s48 offen offset:2048 sc1 lds\n\t" \
  "LA_o1_%=:\n\t" \
  "s_add_u32 s48, s48, 5120\n\t" \
  "v_exp_f32 v78, v78\n\t" \
  "v_exp_f32 v79, v79\n\t" \
  "v_exp_f32 v80, v80\n\t" \
  "v_exp_f32 v81, v81\n\t" \
  "v_smfmac_f32_32x32x32_bf16 v[12:27], v[128:131], v[68:75], v136\n\t" \
  "v_exp_f32 v82, v82\n\t" \
  "v_exp_f32 v83, v83\n\t" \
  "v_exp_f32 v52, v52\n\t" \
  "v_exp_f32 v53, v53\n\t" \
  "v_smfmac_f32_32x32x32_bf16 v[12:27], v[132:135], v[76:83], v136\n\t" \
  "v_exp_f32 v54, v54\n\t" \
  "v_exp_f32 v55, v55\n\t" \
  "v_exp_f32 v56, v56\n\t" \
  "v_exp_f32 v57, v57\n\t" \
  "v_mfma_f32_32x32x16_bf16 v[68:83], v[122:125], v[102:105], 0\n\t" \
  "v_exp_f32 v58, v58\n\t" \
  "v_exp_f32 v59, v59\n\t" \
  "v_exp_f32 v60, v60\n\t" \
  "v_exp_f32 v61, v61\n\t" \
  "v_mfma_f32_32x32x8_bf16 v[68:83], v[126:127], v[106:107], v[68:83]\n\t" \
  "s_add_u32 m0, %[lw], 94208\n\t" \
  "s_nop 0\n\t" \
  "buffer_load_dwordx4 v236, %[rs], s48 offen sc1 lds\n\t" \
  "buffer_load_dwordx4 v236, %[rs], s48 offen offset:1024 sc1 lds\n\t" \
  "s_cmp_eq_u32 %[th], 0\n\t" \
  "s_cbranch_scc0 LA_o2_%=\n\t" \
  "buffer_load_dwordx4 v236, %[rs], s48 offen offset:2048 sc1 lds\n\t" \
  "LA_o2_%=:\n\t" \
  "s_add_u32 s48, s48, 5120\n\t" \
  "v_exp_f32 v62, v62\n\t" \
  "v_exp_f32 v63, v63\n\t" \
  "v_exp_f32 v64, v64\n\t" \
  "v_exp_f32 v65, v65\n\t" \
  "v_smfmac_f32_32x32x32_bf16 v[24:39], v[128:131], v[52:59], v136\n\t" \
  "v_exp_f32 v66, v66\n\t" \
  "v_exp_f32 v67, v67\n\t" \
  "v_exp_f32 v68, v68\n\t" \
  "v_exp_f32 v69, v69\n\t" \
  "v_smfmac_f32_32x32x32_bf16 v[24:39], v[132:135], v[60:67], v136\n\t" \
  "v_exp_f32 v70, v70\n\t" \
  "v_exp_f32 v71, v71\n\t" \
  "v_exp_f32 v72, v72\n\t" \
  "v_exp_f32 v73, v73\n\t" \
  "s_waitcnt vmcnt(6)\n\t" \
  "v_mfma_f32_32x32x16_bf16 v[52:67], v[108:111], v[84:87], 0\n\t" \
  "v_exp_f32 v74, v74\n\t" \
  "v_exp_f32 v75, v75\n\t" \
  "v_exp_f32 v76, v76\n\t" \
  "v_exp_f32 v77, v77\n\t" \
  "v_mfma_f32_32x32x8_bf16 v[52:67], v[112:113], v[88:89], v[52:67]\n\t" \
  "buffer_load_dwordx4 v[122:125], v236, %[rs], s40 offen sc1\n\t" \
  "buffer_load_dwordx2 v[126:127], v237, %[rs], s40 offen offset:3072 sc1\n\t" \
  "s_add_u32 m0, %[lw], 114688\n\t" \
  "s_nop 0\n\t" \
  "buffer_load_dwordx4 v236, %[rs], s48 offen sc1 lds\n\t" \
  "buffer_load_dwordx4 v236, %[rs], s48 offen offset:1024 sc1 lds\n\t" \
  "s_cmp_eq_u32 %[th], 0\n\t" \
  "s_cbranch_scc0 LA_o3_%=\n\t" \
  "buffer_load_dwordx4 v236, %[rs], s48 offen offset:2048 sc1 lds\n\t" \
  "LA_o3_%=:\n\t" \
  "s_add_u32 s48, s48, 5120\n\t" \
  "v_exp_f32 v78, v78\n\t" \
  "v_exp_f32 v79, v79\n\t" \
  "v_exp_f32 v80, v80\n\t" \
  "v_exp_f32 v81, v81\n\t" \
  "v_smfmac_f32_32x32x32_bf16 v[36:51], v[128:131], v[68:75], v136\n\t" \
  "v_exp_f32 v82, v82\n\t" \
  "v_exp_f32 v83, v83\n\t" \
  "v_exp_f32 v52, v52\n\t" \
  "v_exp_f32 v53, v53\n\t" \
  "v_smfmac_f32_32x32x32_bf16 v[36:51], v[132:135], v[76:83], v136\n\t" \
  "v_exp_f32 v54, v54\n\t" \
  "v_exp_f32 v55, v55\n\t" \
  "v_exp_f32 v56, v56\n\t" \
  "v_exp_f32 v57, v57\n\t" \
  "v_mfma_f32_32x32x16_bf16 v[68:83], v[108:111], v[90:93], 0\n\t" \
  "v_exp_f32 v58, v58\n\t" \
  "v_exp_f32 v59, v59\n\t" \
  "v_exp_f32 v60, v60\n\t" \
  "v_exp_f32 v61, v61\n\t" \
  "v_mfma_f32_32x32x8_bf16 v[68:83], v[112:113], v[94:95], v[68:83]\n\t" \
  "s_add_u32 m0, %[lw], 135168\n\t" \
  "s_nop 0\n\t" \
  "buffer_load_dwordx4 v236, %[rs], s48 offen sc1 lds\n\t" \
  "buffer_load_dwordx4 v236, %[rs], s48 offen offset:1024 sc1 lds\n\t" \
  "s_cmp_eq_u32 %[th], 0\n\t" \
  "s_cbranch_scc0 LA_o4_%=\n\t" \
  "buffer_load_dwordx4 v236, %[rs], s48 offen offset:2048 sc1 lds\n\t" \
  "LA_o4_%=:\n\t" \
  "s_add_u32 s48, s48, 5120\n\t" \
  "v_exp_f32 v62, v62\n\t" \
  "v_exp_f32 v63, v63\n\t" \
  "v_exp_f32 v64, v64\n\t" \
  "v_exp_f32 v65, v65\n\t" \
  "s_waitcnt vmcnt(10)\n\t" \
  "v_smfmac_f32_32x32x32_bf16 v[0:15], v[114:117], v[52:59], v136\n\t" \
  "buffer_load_dwordx4 v[128:131], v238, %[rs], s40 offen offset:1024 sc1\n\t" \
  "buffer_load_dwordx4 v[132:135], v238, %[rs], s40 offen offset:2048 sc1\n\t" \
  "s_add_u32 s40, s40, 3584\n\t" \
  "s_cmp_eq_u32 s40, s41\n\t" \
  "s_cselect_b32 s40, %[kb], s40\n\t" \
  "v_exp_f32 v66, v66\n\t" \
  "v_exp_f32 v67, v67\n\t" \
  "v_exp_f32 v68, v68\n\t" \
  "v_exp_f32 v69, v69\n\t" \
  "v_smfmac_f32_32x32x32_bf16 v[0:15], v[118:121], v[60:67], v136\n\t" \
  "v_exp_f32 v70, v70\n\t" \
  "v_exp_f32 v71, v71\n\t" \
  "v_exp_f32 v72, v72\n\t" \
  "v_exp_f32 v73, v73\n\t" \
  "v_mfma_f32_32x32x16_bf16 v[52:67], v[108:111], v[96:99], 0\n\t" \
  "v_exp_f32 v74, v74\n\t" \
  "v_exp_f32 v75, v75\n\t" \
  "v_exp_f32 v76, v76\n\t" \
  "v_exp_f32 v77, v77\n\t" \
  "v_mfma_f32_32x32x8_bf16 v[52:67], v[112:113], v[100:101], v[52:67]\n\t" \
  "buffer_load_dwordx4 v[140:143], v236, %[rs], s46 offen sc1\n\t" \
  "buffer_load_dwordx4 v[144:147], v236, %[rs], s46 offen offset:1024 sc1\n\t" \
  "s_add_u32 s46, s46, 4096\n\t" \
  "v_exp_f32 v78, v78\n\t" \
  "v_exp_f32 v79, v79\n\t" \
  "v_exp_f32 v80, v80\n\t" \
  "v_exp_f32 v81, v81\n\t" \
  "v_smfmac_f32_32x32x32_bf16 v[12:27], v[114:117], v[68:75], v136\n\t" \
  "v_exp_f32 v82, v82\n\t" \
  "v_exp_f32 v83, v83\n\t" \
  "v_exp_f32 v52, v52\n\t" \
  "v_exp_f32 v53, v53\n\t" \
  "v_smfmac_f32_32x32x32_bf16 v[12:27], v[118:121], v[76:83], v136\n\t" \
  "v_exp_f32 v54, v54\n\t" \
  "v_exp_f32 v55, v55\n\t" \
  "v_exp_f32 v56, v56\n\t" \
  "v_exp_f32 v57, v57\n\t" \
  "v_mfma_f32_32x32x16_bf16 v[68:83], v[108:111], v[102:105], 0\n\t" \
  "v_exp_f32 v58, v58\n\t" \
  "v_exp_f32 v59, v59\n\t" \
  "v_exp_f32 v60, v60\n\t" \
  "v_exp_f32 v61, v61\n\t" \
  "v_mfma_f32_32x32x8_bf16 v[68:83], v[112:113], v[106:107], v[68:83]\n\t" \
  "buffer_load_dwordx4 v[148:151], v236, %[rs], s46 offen sc1\n\t" \
  "buffer_load_dwordx4 v[152:155], v236, %[rs], s46 offen offset:1024 sc1\n\t" \
  "s_add_u32 s46, s46, 4096\n\t" \
  "v_exp_f32 v62, v62\n\t" \
  "v_exp_f32 v63, v63\n\t" \
  "v_exp_f32 v64, v64\n\t" \
  "v_exp_f32 v65, v65\n\t" \
  "v_smfmac_f32_32x32x32_bf16 v[24:39], v[114:117], v[52:59], v136\n\t" \
  "v_exp_f32 v66, v66\n\t" \
  "v_exp_f32 v67, v67\n\t" \
  "v_exp_f32 v68, v68\n\t" \
  "v_exp_f32 v69, v69\n\t" \
  "v_smfmac_f32_32x32x32_bf16 v[24:39], v[118:121], v[60:67], v136\n\t" \
  "v_exp_f32 v70, v70\n\t" \
  "v_exp_f32 v71, v71\n\t" \
  "v_exp_f32 v72, v72\n\t" \
  "v_exp_f32 v73, v73\n\t" \
  "s_waitcnt vmcnt(10)\n\t" \
  "v_mfma_f32_32x32x16_bf16 v[52:67], v[122:125], v[84:87], 0\n\t" \
  "v_exp_f32 v74, v74\n\t" \
  "v_exp_f32 v75, v75\n\t" \
  "v_exp_f32 v76, v76\n\t" \
  "v_exp_f32 v77, v77\n\t" \
  "v_mfma_f32_32x32x8_bf16 v[52:67], v[126:127], v[88:89], v[52:67]\n\t" \
  "buffer_load_dwordx4 v[108:111], v236, %[rs], s40 offen sc1\n\t" \
  "buffer_load_dwordx2 v[112:113], v237, %[rs], s40 offen offset:3072 sc1\n\t" \
  "buffer_load_dwordx4 v[156:159], v236, %[rs], s46 offen sc1\n\t" \
  "buffer_load_dwordx4 v[160:163], v236, %[rs], s46 offen offset:1024 sc1\n\t" \
  "s_add_u32 s46, s46, 4096\n\t" \
  "v_exp_f32 v78, v78\n\t" \
  "v_exp_f32 v79, v79\n\t" \
  "v_exp_f32 v80, v80\n\t" \
  "v_exp_f32 v81, v81\n\t" \
  "v_smfmac_f32_32x32x32_bf16 v[36:51], v[114:117], v[68:75], v136\n\t" \
  "v_exp_f32 v82, v82\n\t" \
  "v_exp_f32 v83, v83\n\t" \
  "v_exp_f32 v52, v52\n\t" \
  "v_exp_f32 v53, v53\n\t" \
  "v_smfmac_f32_32x32x32_bf16 v[36:51], v[118:121], v[76:83], v136\n\t" \
  "v_exp_f32 v54, v54\n\t" \
  "v_exp_f32 v55, v55\n\t" \
  "v_exp_f32 v56, v56\n\t" \
  "v_exp_f32 v57, v57\n\t" \
  "v_mfma_f32_32x32x16_bf16 v[68:83], v[122:125], v[90:93], 0\n\t" \
  "v_exp_f32 v58, v58\n\t" \
  "v_exp_f32 v59, v59\n\t" \
  "v_exp_f32 v60, v60\n\t" \
  "v_exp_f32 v61, v61\n\t" \
  "v_mfma_f32_32x32x8_bf16 v[68:83], v[126:127], v[94:95], v[68:83]\n\t" \
  "buffer_load_dwordx4 v[164:167], v236, %[rs], s46 offen sc1\n\t" \
  "buffer_load_dwordx4 v[168:171], v236, %[rs], s46 offen offset:1024 sc1\n\t" \
  "s_add_u32 s46, s46, 4096\n\t" \
  "v_exp_f32 v62, v62\n\t" \
  "v_exp_f32 v63, v63\n\t" \
  "v_exp_f32 v64, v64\n\t" \
  "v_exp_f32 v65, v65\n\t" \
  "s_waitcnt vmcnt(10)\n\t" \
  "v_smfmac_f32_32x32x32_bf16 v[0:15], v[128:131], v[52:59], v136\n\t" \
  "buffer_load_dwordx4 v[114:117], v238, %[rs], s40 offen offset:1024 sc1\n\t" \
  "buffer_load_dwordx4 v[118:121], v238, %[rs], s40 offen offset:2048 sc1\n\t" \
  "s_add_u32 s40, s40, 3584\n\t" \
  "s_cmp_eq_u32 s40, s41\n\t" \
  "s_cselect_b32 s40, %[kb], s40\n\t" \
  "v_exp_f32 v66, v66\n\t" \
  "v_exp_f32 v67, v67\n\t" \
  "v_exp_f32 v68, v68\n\t" \
  "v_exp_f32 v69, v69\n\t" \
  "v_smfmac_f32_32x32x32_bf16 v[0:15], v[132:135], v[60:67], v136\n\t" \
  "v_exp_f32 v70, v70\n\t" \
  "v_exp_f32 v71, v71\n\t" \
  "v_exp_f32 v72, v72\n\t" \
  "v_exp_f32 v73, v73\n\t" \
  "v_mfma_f32_32x32x16_bf16 v[52:67], v[122:125], v[96:99], 0\n\t" \
  "v_exp_f32 v74, v74\n\t" \
  "v_exp_f32 v75, v75\n\t" \
  "v_exp_f32 v76, v76\n\t" \
  "v_exp_f32 v77, v77\n\t" \
  "v_mfma_f32_32x32x8_bf16 v[52:67], v[126:127], v[100:101], v[52:67]\n\t" \
  "buffer_load_dwordx4 v[172:175], v236, %[rs], s46 offen sc1\n\t" \
  "buffer_load_dwordx4 v[176:179], v236, %[rs], s46 offen offset:1024 sc1\n\t" \
  "s_add_u32 s46, s46, 4096\n\t" \
  "v_exp_f32 v78, v78\n\t" \
  "v_exp_f32 v79, v79\n\t" \
  "v_exp_f32 v80, v80\n\t" \
  "v_exp_f32 v81, v81\n\t" \
  "v_smfmac_f32_32x32x32_bf16 v[12:27], v[128:131], v[68:75], v136\n\t" \
  "v_exp_f32 v82, v82\n\t" \
  "v_exp_f32 v83, v83\n\t" \
  "v_exp_f32 v52, v52\n\t" \
  "v_exp_f32 v53, v53\n\t" \
  "v_smfmac_f32_32x32x32_bf16 v[12:27], v[132:135], v[76:83], v136\n\t" \
  "v_exp_f32 v54, v54\n\t" \
  "v_exp_f32 v55, v55\n\t" \
  "v_exp_f32 v56, v56\n\t" \
  "v_exp_f32 v57, v57\n\t" \
  "v_mfma_f32_32x32x16_bf16 v[68:83], v[122:125], v[102:105], 0\n\t" \
  "v_exp_f32 v58, v58\n\t" \
  "v_exp_f32 v59, v59\n\t" \
  "v_exp_f32 v60, v60\n\t" \
  "v_exp_f32 v61, v61\n\t" \
  "v_mfma_f32_32x32x8_bf16 v[68:83], v[126:127], v[106:107], v[68:83]\n\t" \
  "buffer_load_dwordx4 v[180:183], v236, %[rs], s46 offen sc1\n\t" \
  "buffer_load_dwordx4 v[184:187], v236, %[rs], s46 offen offset:1024 sc1\n\t" \
  "s_add_u32 s46, s46, 4096\n\t" \
  "v_exp_f32 v62, v62\n\t" \
  "v_exp_f32 v63, v63\n\t" \
  "v_exp_f32 v64, v64\n\t" \
  "v_exp_f32 v65, v65\n\t" \
  "v_smfmac_f32_32x32x32_bf16 v[24:39], v[128:131], v[52:59], v136\n\t" \
  "v_exp_f32 v66, v66\n\t" \
  "v_exp_f32 v67, v67\n\t" \
  "v_exp_f32 v68, v68\n\t" \
  "v_exp_f32 v69, v69\n\t" \
  "v_smfmac_f32_32x32x32_bf16 v[24:39], v[132:135], v[60:67], v136\n\t" \
  "v_exp_f32 v70, v70\n\t" \
  "v_exp_f32 v71, v71\n\t" \
  "v_exp_f32 v72, v72\n\t" \
  "v_exp_f32 v73, v73\n\t" \
  "s_waitcnt vmcnt(10)\n\t" \
  "v_mfma_f32_32x32x16_bf16 v[52:67], v[108:111], v[84:87], 0\n\t" \
  "v_exp_f32 v74, v74\n\t" \
  "v_exp_f32 v75, v75\n\t" \
  "v_exp_f32 v76, v76\n\t" \
  "v_exp_f32 v77, v77\n\t" \
  "v_mfma_f32_32x32x8_bf16 v[52:67], v[112:113], v[88:89], v[52:67]\n\t" \
  "buffer_load_dwordx4 v[122:125], v236, %[rs], s40 offen sc1\n\t" \
  "buffer_load_dwordx2 v[126:127], v237, %[rs], s40 offen offset:3072 sc1\n\t" \
  "buffer_load_dwordx4 v[188:191], v236, %[rs], s46 offen sc1\n\t" \
  "buffer_load_dwordx4 v[192:195], v236, %[rs], s46 offen offset:1024 sc1\n\t" \
  "s_add_u32 s46, s46, 4096\n\t" \
  "v_exp_f32 v78, v78\n\t" \
  "v_exp_f32 v79, v79\n\t" \
  "v_exp_f32 v80, v80\n\t" \
  "v_exp_f32 v81, v81\n\t" \
  "v_smfmac_f32_32x32x32_bf16 v[36:51], v[128:131], v[68:75], v136\n\t" \
  "v_exp_f32 v82, v82\n\t" \
  "v_exp_f32 v83, v83\n\t" \
  "v_exp_f32 v52, v52\n\t" \
  "v_exp_f32 v53, v53\n\t" \
  "v_smfmac_f32_32x32x32_bf16 v[36:51], v[132:135], v[76:83], v136\n\t" \
  "v_exp_f32 v54, v54\n\t" \
  "v_exp_f32 v55, v55\n\t" \
  "v_exp_f32 v56, v56\n\t" \
  "v_exp_f32 v57, v57\n\t" \
  "v_mfma_f32_32x32x16_bf16 v[68:83], v[108:111], v[90:93], 0\n\t" \
  "v_exp_f32 v58, v58\n\t" \
  "v_exp_f32 v59, v59\n\t" \
  "v_exp_f32 v60, v60\n\t" \
  "v_exp_f32 v61, v61\n\t" \
  "v_mfma_f32_32x32x8_bf16 v[68:83], v[112:113], v[94:95], v[68:83]\n\t" \
  "buffer_load_dwordx4 v[196:199], v236, %[rs], s46 offen sc1\n\t" \
  "buffer_load_dwordx4 v[200:203], v236, %[rs], s46 offen offset:1024 sc1\n\t" \
  "s_add_u32 s46, s46, 4096\n\t" \
  "v_exp_f32 v62, v62\n\t" \
  "v_exp_f32 v63, v63\n\t" \
  "v_exp_f32 v64, v64\n\t" \
  "v_exp_f32 v65, v65\n\t" \
  "s_waitcnt vmcnt(10)\n\t" \
  "v_smfmac_f32_32x32x32_bf16 v[0:15], v[114:117], v[52:59], v136\n\t" \
  "buffer_load_dwordx4 v[128:131], v238, %[rs], s40 offen offset:1024 sc1\n\t" \
  "buffer_load_dwordx4 v[132:135], v238, %[rs], s40 offen offset:2048 sc1\n\t" \
  "s_add_u32 s40, s40, 3584\n\t" \
  "s_cmp_eq_u32 s40, s41\n\t" \
  "s_cselect_b32 s40, %[kb], s40\n\t" \
  "v_exp_f32 v66, v66\n\t" \
  "v_exp_f32 v67, v67\n\t" \
  "v_exp_f32 v68, v68\n\t" \
  "v_exp_f32 v69, v69\n\t" \
  "v_smfmac_f32_32x32x32_bf16 v[0:15], v[118:121], v[60:67], v136\n\t" \
  "v_exp_f32 v70, v70\n\t" \
  "v_exp_f32 v71, v71\n\t" \
  "v_exp_f32 v72, v72\n\t" \
  "v_exp_f32 v73, v73\n\t" \
  "v_mfma_f32_32x32x16_bf16 v[52:67], v[108:111], v[96:99], 0\n\t" \
  "v_exp_f32 v74, v74\n\t" \
  "v_exp_f32 v75, v75\n\t" \
  "v_exp_f32 v76, v76\n\t" \
  "v_exp_f32 v77, v77\n\t" \
  "v_mfma_f32_32x32x8_bf16 v[52:67], v[112:113], v[100:101], v[52:67]\n\t" \
  "buffer_load_dwordx4 v[204:207], v236, %[rs], s46 offen sc1\n\t" \
  "buffer_load_dwordx4 v[208:211], v236, %[rs], s46 offen offset:1024 sc1\n\t" \
  "s_add_u32 s46, s46, 4096\n\t" \
  "v_exp_f32 v78, v78\n\t" \
  "v_exp_f32 v79, v79\n\t" \
  "v_exp_f32 v80, v80\n\t" \
  "v_exp_f32 v81, v81\n\t" \
  "v_smfmac_f32_32x32x32_bf16 v[12:27], v[114:117], v[68:75], v136\n\t" \
  "v_exp_f32 v82, v82\n\t" \
  "v_exp_f32 v83, v83\n\t" \
  "v_exp_f32 v52, v52\n\t" \
  "v_exp_f32 v53, v53\n\t" \
  "v_smfmac_f32_32x32x32_bf16 v[12:27], v[118:121], v[76:83], v136\n\t" \
  "v_exp_f32 v54, v54\n\t" \
  "v_exp_f32 v55, v55\n\t" \
  "v_exp_f32 v56, v56\n\t" \
  "v_exp_f32 v57, v57\n\t" \
  "v_mfma_f32_32x32x16_bf16 v[68:83], v[108:111], v[102:105], 0\n\t" \
  "v_exp_f32 v58, v58\n\t" \
  "v_exp_f32 v59, v59\n\t" \
  "v_exp_f32 v60, v60\n\t" \
  "v_exp_f32 v61, v61\n\t" \
  "v_mfma_f32_32x32x8_bf16 v[68:83], v[112:113], v[106:107], v[68:83]\n\t" \
  "buffer_load_dwordx4 v[212:215], v236, %[rs], s46 offen sc1\n\t" \
  "buffer_load_dwordx4 v[216:219], v236, %[rs], s46 offen offset:1024 sc1\n\t" \
  "s_add_u32 s46, s46, 4096\n\t" \
  "v_exp_f32 v62, v62\n\t" \
  "v_exp_f32 v63, v63\n\t" \
  "v_exp_f32 v64, v64\n\t" \
  "v_exp_f32 v65, v65\n\t" \
  "v_smfmac_f32_32x32x32_bf16 v[24:39], v[114:117], v[52:59], v136\n\t" \
  "v_exp_f32 v66, v66\n\t" \
  "v_exp_f32 v67, v67\n\t" \
  "v_exp_f32 v68, v68\n\t" \
  "v_exp_f32 v69, v69\n\t" \
  "v_smfmac_f32_32x32x32_bf16 v[24:39], v[118:121], v[60:67], v136\n\t" \
  "v_exp_f32 v70, v70\n\t" \
  "v_exp_f32 v71, v71\n\t" \
  "v_exp_f32 v72, v72\n\t" \
  "v_exp_f32 v73, v73\n\t" \
  "s_waitcnt vmcnt(10)\n\t" \
  "v_mfma_f32_32x32x16_bf16 v[52:67], v[122:125], v[84:87], 0\n\t" \
  "v_exp_f32 v74, v74\n\t" \
  "v_exp_f32 v75, v75\n\t" \
  "v_exp_f32 v76, v76\n\t" \
  "v_exp_f32 v77, v77\n\t" \
  "v_mfma_f32_32x32x8_bf16 v[52:67], v[126:127], v[88:89], v[52:67]\n\t" \
  "buffer_load_dwordx4 v[108:111], v236, %[rs], s40 offen sc1\n\t" \
  "buffer_load_dwordx2 v[112:113], v237, %[rs], s40 offen offset:3072 sc1\n\t" \
  "buffer_load_dwordx4 v[220:223], v236, %[rs], s46 offen sc1\n\t" \
  "buffer_load_dwordx4 v[224:227], v236, %[rs], s46 offen offset:1024 sc1\n\t" \
  "s_add_u32 s46, s46, 4096\n\t" \
  "v_exp_f32 v78, v78\n\t" \
  "v_exp_f32 v79, v79\n\t" \
  "v_exp_f32 v80, v80\n\t" \
  "v_exp_f32 v81, v81\n\t" \
  "v_smfmac_f32_32x32x32_bf16 v[36:51], v[114:117], v[68:75], v136\n\t" \
  "v_exp_f32 v82, v82\n\t" \
  "v_exp_f32 v83, v83\n\t" \
  "v_exp_f32 v52, v52\n\t" \
  "v_exp_f32 v53, v53\n\t" \
  "v_smfmac_f32_32x32x32_bf16 v[36:51], v[118:121], v[76:83], v136\n\t" \
  "v_exp_f32 v54, v54\n\t" \
  "v_exp_f32 v55, v55\n\t" \
  "v_exp_f32 v56, v56\n\t" \
  "v_exp_f32 v57, v57\n\t" \
  "v_mfma_f32_32x32x16_bf16 v[68:83], v[122:125], v[90:93], 0\n\t" \
  "v_exp_f32 v58, v58\n\t" \
  "v_exp_f32 v59, v59\n\t" \
  "v_exp_f32 v60, v60\n\t" \
  "v_exp_f32 v61, v61\n\t" \
  "v_mfma_f32_32x32x8_bf16 v[68:83], v[126:127], v[94:95], v[68:83]\n\t" \
  "buffer_load_dwordx4 v[228:231], v236, %[rs], s46 offen sc1\n\t" \
  "buffer_load_dwordx4 v[232:235], v236, %[rs], s46 offen offset:1024 sc1\n\t" \
  "s_add_u32 s46, s46, 4096\n\t" \
  "v_exp_f32 v62, v62\n\t" \
  "v_exp_f32 v63, v63\n\t" \
  "v_exp_f32 v64, v64\n\t" \
  "v_exp_f32 v65, v65\n\t" \
  "s_waitcnt vmcnt(10)\n\t" \
  "v_smfmac_f32_32x32x32_bf16 v[0:15], v[128:131], v[52:59], v136\n\t" \
  "buffer_load_dwordx4 v[114:117], v238, %[rs], s40 offen offset:1024 sc1\n\t" \
  "buffer_load_dwordx4 v[118:121], v238, %[rs], s40 offen offset:2048 sc1\n\t" \
  "s_add_u32 s40, s40, 3584\n\t" \
  "s_cmp_eq_u32 s40, s41\n\t" \
  "s_cselect_b32 s40, %[kb], s40\n\t" \
  "v_exp_f32 v66, v66\n\t" \
  "v_exp_f32 v67, v67\n\t" \
  "v_exp_f32 v68, v68\n\t" \
  "v_exp_f32 v69, v69\n\t" \
  "v_smfmac_f32_32x32x32_bf16 v[0:15], v[132:135], v[60:67], v136\n\t" \
  "v_exp_f32 v70, v70\n\t" \
  "v_exp_f32 v71, v71\n\t" \
  "v_exp_f32 v72, v72\n\t" \
  "v_exp_f32 v73, v73\n\t" \
  "v_mfma_f32_32x32x16_bf16 v[52:67], v[122:125], v[96:99], 0\n\t" \
  "v_exp_f32 v74, v74\n\t" \
  "v_exp_f32 v75, v75\n\t" \
  "v_exp_f32 v76, v76\n\t" \
  "v_exp_f32 v77, v77\n\t" \
  "v_mfma_f32_32x32x8_bf16 v[52:67], v[126:127], v[100:101], v[52:67]\n\t" \
  "v_exp_f32 v78, v78\n\t" \
  "v_exp_f32 v79, v79\n\t" \
  "v_exp_f32 v80, v80\n\t" \
  "v_exp_f32 v81, v81\n\t" \
  "v_smfmac_f32_32x32x32_bf16 v[12:27], v[128:131], v[68:75], v136\n\t" \
  "v_exp_f32 v82, v82\n\t" \
  "v_exp_f32 v83, v83\n\t" \
  "v_exp_f32 v52, v52\n\t" \
  "v_exp_f32 v53, v53\n\t" \
  "v_smfmac_f32_32x32x32_bf16 v[12:27], v[132:135], v[76:83], v136\n\t" \
  "v_exp_f32 v54, v54\n\t" \
  "v_exp_f32 v55, v55\n\t" \
  "v_exp_f32 v56, v56\n\t" \
  "v_exp_f32 v57, v57\n\t" \
  "s_setprio 0\n\t" \
  "v_mfma_f32_32x32x16_bf16 v[68:83], v[122:125], v[102:105], 0\n\t" \
  "v_exp_f32 v58, v58\n\t" \
  "v_exp_f32 v59, v59\n\t" \
  "v_exp_f32 v60, v60\n\t" \
  "v_exp_f32 v61, v61\n\t" \
  "v_mfma_f32_32x32x8_bf16 v[68:83], v[126:127], v[106:107], v[68:83]\n\t" \
  "v_exp_f32 v62, v62\n\t" \
  "v_exp_f32 v63, v63\n\t" \
  "v_exp_f32 v64, v64\n\t" \
  "v_exp_f32 v65, v65\n\t" \
  "v_smfmac_f32_32x32x32_bf16 v[24:39], v[128:131], v[52:59], v136\n\t" \
  "v_exp_f32 v66, v66\n\t" \
  "v_exp_f32 v67, v67\n\t" \
  "v_exp_f32 v68, v68\n\t" \
  "v_exp_f32 v69, v69\n\t" \
  "v_smfmac_f32_32x32x32_bf16 v[24:39], v[132:135], v[60:67], v136\n\t" \
  "v_exp_f32 v70, v70\n\t" \
  "v_exp_f32 v71, v71\n\t" \
  "v_exp_f32 v72, v72\n\t" \
  "v_exp_f32 v73, v73\n\t" \
  "s_waitcnt vmcnt(6)\n\t" \
  "v_mfma_f32_32x32x16_bf16 v[52:67], v[108:111], v[84:87], 0\n\t" \
  "v_exp_f32 v74, v74\n\t" \
  "v_exp_f32 v75, v75\n\t" \
  "v_exp_f32 v76, v76\n\t" \
  "v_exp_f32 v77, v77\n\t" \
  "v_mfma_f32_32x32x8_bf16 v[52:67], v[112:113], v[88:89], v[52:67]\n\t" \
  "buffer_load_dwordx4 v[122:125], v236, %[rs], s40 offen sc1\n\t" \
  "buffer_load_dwordx2 v[126:127], v237, %[rs], s40 offen offset:3072 sc1\n\t" \
  "v_exp_f32 v78, v78\n\t" \
  "v_exp_f32 v79, v79\n\t" \
  "v_exp_f32 v80, v80\n\t" \
  "v_exp_f32 v81, v81\n\t" \
  "v_smfmac_f32_32x32x32_bf16 v[36:51], v[128:131], v[68:75], v136\n\t" \
  "v_exp_f32 v82, v82\n\t" \
  "v_exp_f32 v83, v83\n\t" \
  "v_exp_f32 v52, v52\n\t" \
  "v_exp_f32 v53, v53\n\t" \
  "v_smfmac_f32_32x32x32_bf16 v[36:51], v[132:135], v[76:83], v136\n\t" \
  "v_exp_f32 v54, v54\n\t" \
  "v_exp_f32 v55, v55\n\t" \
  "v_exp_f32 v56, v56\n\t" \
  "v_exp_f32 v57, v57\n\t" \
  "v_mfma_f32_32x32x16_bf16 v[68:83], v[108:111], v[90:93], 0\n\t" \
  "v_exp_f32 v58, v58\n\t" \
  "v_exp_f32 v59, v59\n\t" \
  "v_exp_f32 v60, v60\n\t" \
  "v_exp_f32 v61, v61\n\t" \
  "v_mfma_f32_32x32x8_bf16 v[68:83], v[112:113], v[94:95], v[68:83]\n\t" \
  "v_exp_f32 v62, v62\n\t" \
  "v_exp_f32 v63, v63\n\t" \
  "v_exp_f32 v64, v64\n\t" \
  "v_exp_f32 v65, v65\n\t" \
  "s_waitcnt vmcnt(2)\n\t" \
  "v_smfmac_f32_32x32x32_bf16 v[0:15], v[114:117], v[52:59], v136\n\t" \
  "buffer_load_dwordx4 v[128:131], v238, %[rs], s40 offen offset:1024 sc1\n\t" \
  "buffer_load_dwordx4 v[132:135], v238, %[rs], s40 offen offset:2048 sc1\n\t" \
  "s_add_u32 s40, s40, 3584\n\t" \
  "s_cmp_eq_u32 s40, s41\n\t" \
  "s_cselect_b32 s40, %[kb], s40\n\t" \
  "v_exp_f32 v66, v66\n\t" \
  "v_exp_f32 v67, v67\n\t" \
  "v_exp_f32 v68, v68\n\t" \
  "v_exp_f32 v69, v69\n\t" \
  "v_smfmac_f32_32x32x32_bf16 v[0:15], v[118:121], v[60:67], v136\n\t" \
  "v_exp_f32 v70, v70\n\t" \
  "v_exp_f32 v71, v71\n\t" \
  "v_exp_f32 v72, v72\n\t" \
  "v_exp_f32 v73, v73\n\t" \
  "v_mfma_f32_32x32x16_bf16 v[52:67], v[108:111], v[96:99], 0\n\t" \
  "v_exp_f32 v74, v74\n\t" \
  "v_exp_f32 v75, v75\n\t" \
  "v_exp_f32 v76, v76\n\t" \
  "v_exp_f32 v77, v77\n\t" \
  "v_mfma_f32_32x32x8_bf16 v[52:67], v[112:113], v[100:101], v[52:67]\n\t" \
  "v_exp_f32 v78, v78\n\t" \
  "v_exp_f32 v79, v79\n\t" \
  "v_exp_f32 v80, v80\n\t" \
  "v_exp_f32 v81, v81\n\t" \
  "v_smfmac_f32_32x32x32_bf16 v[12:27], v[114:117], v[68:75], v136\n\t" \
  "v_exp_f32 v82, v82\n\t" \
  "v_exp_f32 v83, v83\n\t" \
  "v_exp_f32 v52, v52\n\t" \
  "v_exp_f32 v53, v53\n\t" \
  "v_smfmac_f32_32x32x32_bf16 v[12:27], v[118:121], v[76:83], v136\n\t" \
  "v_exp_f32 v54, v54\n\t" \
  "v_exp_f32 v55, v55\n\t" \
  "v_exp_f32 v56, v56\n\t" \
  "v_exp_f32 v57, v57\n\t" \
  "v_mfma_f32_32x32x16_bf16 v[68:83], v[108:111], v[102:105], 0\n\t" \
  "v_exp_f32 v58, v58\n\t" \
  "v_exp_f32 v59, v59\n\t" \
  "v_exp_f32 v60, v60\n\t" \
  "v_exp_f32 v61, v61\n\t" \
  "v_mfma_f32_32x32x8_bf16 v[68:83], v[112:113], v[106:107], v[68:83]\n\t" \
  "v_exp_f32 v62, v62\n\t" \
  "v_exp_f32 v63, v63\n\t" \
  "v_exp_f32 v64, v64\n\t" \
  "v_exp_f32 v65, v65\n\t" \
  "v_smfmac_f32_32x32x32_bf16 v[24:39], v[114:117], v[52:59], v136\n\t" \
  "v_exp_f32 v66, v66\n\t" \
  "v_exp_f32 v67, v67\n\t" \
  "v_exp_f32 v68, v68\n\t" \
  "v_exp_f32 v69, v69\n\t" \
  "v_smfmac_f32_32x32x32_bf16 v[24:39], v[118:121], v[60:67], v136\n\t" \
  "v_exp_f32 v70, v70\n\t" \
  "v_exp_f32 v71, v71\n\t" \
  "v_exp_f32 v72, v72\n\t" \
  "v_exp_f32 v73, v73\n\t" \
  "s_waitcnt vmcnt(2)\n\t" \
  "v_mfma_f32_32x32x16_bf16 v[52:67], v[122:125], v[84:87], 0\n\t" \
  "v_exp_f32 v74, v74\n\t" \
  "v_exp_f32 v75, v75\n\t" \
  "v_exp_f32 v76, v76\n\t" \
  "v_exp_f32 v77, v77\n\t" \
  "v_mfma_f32_32x32x8_bf16 v[52:67], v[126:127], v[88:89], v[52:67]\n\t" \
  "v_exp_f32 v78, v78\n\t" \
  "v_exp_f32 v79, v79\n\t" \
  "v_exp_f32 v80, v80\n\t" \
  "v_exp_f32 v81, v81\n\t" \
  "v_smfmac_f32_32x32x32_bf16 v[36:51], v[114:117], v[68:75], v136\n\t" \
  "v_exp_f32 v82, v82\n\t" \
  "v_exp_f32 v83, v83\n\t" \
  "v_exp_f32 v52, v52\n\t" \
  "v_exp_f32 v53, v53\n\t" \
  "v_smfmac_f32_32x32x32_bf16 v[36:51], v[118:121], v[76:83], v136\n\t" \
  "v_exp_f32 v54, v54\n\t" \
  "v_exp_f32 v55, v55\n\t" \
  "v_exp_f32 v56, v56\n\t" \
  "v_exp_f32 v57, v57\n\t" \
  "v_mfma_f32_32x32x16_bf16 v[68:83], v[122:125], v[90:93], 0\n\t" \
  "v_exp_f32 v58, v58\n\t" \
  "v_exp_f32 v59, v59\n\t" \
  "v_exp_f32 v60, v60\n\t" \
  "v_exp_f32 v61, v61\n\t" \
  "v_mfma_f32_32x32x8_bf16 v[68:83], v[126:127], v[94:95], v[68:83]\n\t" \
  "v_exp_f32 v62, v62\n\t" \
  "v_exp_f32 v63, v63\n\t" \
  "v_exp_f32 v64, v64\n\t" \
  "v_exp_f32 v65, v65\n\t" \
  "s_waitcnt vmcnt(0)\n\t" \
  "v_smfmac_f32_32x32x32_bf16 v[0:15], v[128:131], v[52:59], v136\n\t" \
  "v_exp_f32 v66, v66\n\t" \
  "v_exp_f32 v67, v67\n\t" \
  "v_exp_f32 v68, v68\n\t" \
  "v_exp_f32 v69, v69\n\t" \
  "v_smfmac_f32_32x32x32_bf16 v[0:15], v[132:135], v[60:67], v136\n\t" \
  "v_exp_f32 v70, v70\n\t" \
  "v_exp_f32 v71, v71\n\t" \
  "v_exp_f32 v72, v72\n\t" \
  "v_exp_f32 v73, v73\n\t" \
  "v_mfma_f32_32x32x16_bf16 v[52:67], v[122:125], v[96:99], 0\n\t" \
  "v_exp_f32 v74, v74\n\t" \
  "v_exp_f32 v75, v75\n\t" \
  "v_exp_f32 v76, v76\n\t" \
  "v_exp_f32 v77, v77\n\t" \
  "v_mfma_f32_32x32x8_bf16 v[52:67], v[126:127], v[100:101], v[52:67]\n\t" \
  "v_exp_f32 v78, v78\n\t" \
  "v_exp_f32 v79, v79\n\t" \
  "v_exp_f32 v80, v80\n\t" \
  "v_exp_f32 v81, v81\n\t" \
  "v_smfmac_f32_32x32x32_bf16 v[12:27], v[128:131], v[68:75], v136\n\t" \
  "v_exp_f32 v82, v82\n\t" \
  "v_exp_f32 v83, v83\n\t" \
  "v_exp_f32 v52, v52\n\t" \
  "v_exp_f32 v53, v53\n\t" \
  "v_smfmac_f32_32x32x32_bf16 v[12:27], v[132:135], v[76:83], v136\n\t" \
  "v_exp_f32 v54, v54\n\t" \
  "v_exp_f32 v55, v55\n\t" \
  "v_exp_f32 v56, v56\n\t" \
  "v_exp_f32 v57, v57\n\t" \
  "v_mfma_f32_32x32x16_bf16 v[68:83], v[122:125], v[102:105], 0\n\t" \
  "v_exp_f32 v58, v58\n\t" \
  "v_exp_f32 v59, v59\n\t" \
  "v_exp_f32 v60, v60\n\t" \
  "v_exp_f32 v61, v61\n\t" \
  "v_mfma_f32_32x32x8_bf16 v[68:83], v[126:127], v[106:107], v[68:83]\n\t" \
  "ds_write_b128 v239, v[0:3] offset:0\n\t" \
  "ds_write_b128 v239, v[4:7] offset:1024\n\t" \
  "ds_write_b128 v239, v[8:11] offset:2048\n\t" \
  "v_exp_f32 v62, v62\n\t" \
  "v_exp_f32 v63, v63\n\t" \
  "v_exp_f32 v64, v64\n\t" \
  "v_exp_f32 v65, v65\n\t" \
  "v_smfmac_f32_32x32x32_bf16 v[24:39], v[128:131], v[52:59], v136\n\t" \
  "v_exp_f32 v66, v66\n\t" \
  "v_exp_f32 v67, v67\n\t" \
  "v_exp_f32 v68, v68\n\t" \
  "v_exp_f32 v69, v69\n\t" \
  "v_smfmac_f32_32x32x32_bf16 v[24:39], v[132:135], v[60:67], v136\n\t" \
  "v_exp_f32 v70, v70\n\t" \
  "v_exp_f32 v71, v71\n\t" \
  "v_exp_f32 v72, v72\n\t" \
  "v_exp_f32 v73, v73\n\t" \
  "v_exp_f32 v74, v74\n\t" \
  "v_exp_f32 v75, v75\n\t" \
  "v_exp_f32 v76, v76\n\t" \
  "v_exp_f32 v77, v77\n\t" \
  "ds_write_b128 v239, v[12:15] offset:3072\n\t" \
  "ds_write_b128 v239, v[16:19] offset:4096\n\t" \
  "ds_write_b128 v239, v[20:23] offset:5120\n\t" \
  "v_exp_f32 v78, v78\n\t" \
  "v_exp_f32 v79, v79\n\t" \
  "v_exp_f32 v80, v80\n\t" \
  "v_exp_f32 v81, v81\n\t" \
  "v_smfmac_f32_32x32x32_bf16 v[36:51], v[128:131], v[68:75], v136\n\t" \
  "v_exp_f32 v82, v82\n\t" \
  "v_exp_f32 v83, v83\n\t" \
  "s_nop 1\n\t" \
  "v_smfmac_f32_32x32x32_bf16 v[36:51], v[132:135], v[76:83], v136\n\t" \
  "ds_write_b128 v239, v[24:27] offset:6144\n\t" \
  "ds_write_b128 v239, v[28:31] offset:7168\n\t" \
  "ds_write_b128 v239, v[32:35] offset:8192\n\t" \
  "s_nop 11\n\t" \
  "s_waitcnt vmcnt(0) lgkmcnt(0)\n\t" \
  "s_branch LA_end_%=\n\t" \
  "LA_spin_%=:\n\t" \
  "s_sleep 4\n\t" \
  "global_load_dwordx2 v[138:139], v137, %[wsb] sc1\n\t" \
  "s_sub_u32 s45, s45, 1\n\t" \
  "s_cbranch_scc1 LA_go_%=\n\t" \
  "s_waitcnt vmcnt(0)\n\t" \
  "v_cmp_ne_u64 vcc, %[tag], v[138:139]\n\t" \
  "s_nop 4\n\t" \
  "s_cbranch_vccnz LA_spin_%=\n\t" \
  "s_branch LA_go_%=\n\t" \
  "LA_end_%=:\n\t"
#define ATT_CLOB "v0", "v1", "v2", "v3", "v4", "v5", "v6", "v7", "v8", "v9", "v10", "v11", "v12", "v13", "v14", "v15", "v16", "v17", "v18", "v19", "v20", "v21", "v22", "v23", "v24", "v25", "v26", "v27", "v28", "v29", "v30", "v31", "v32", "v33", "v34", "v35", "v48", "v49", "v50", "v51", "v62", "v63", "v64", "v65", "v66", "v67", "v68", "v69", "v70", "v71", "v72", "v73", "v74", "v75", "v76", "v77", "v78", "v79", "v80", "v81", "v82", "v83", "v84", "v85", "v86", "v87", "v88", "v89", "v90", "v91", "v92", "v93", "v94", "v95", "v96", "v97", "v98", "v99", "v100", "v101", "v102", "v103", "v104", "v105", "v106", "v107", "v108", "v109", "v110", "v111", "v112", "v113", "v114", "v115", "v116", "v117", "v118", "v119", "v120", "v121", "v122", "v123", "v124", "v125", "v126", "v127", "v128", "v129", "v130", "v131", "v132", "v133", "v134", "v135", "v136", "v138", "v139", "s40", "s41", "s42", "s43", "s44", "s45", "s46", "s47", "s48", "s49", "s50", "s51", "s52", "s53", "vcc", "scc", "memory"
#define DOWN_ASM \
  "s_mov_b64 s[48:49], %[gw]\n\t" \
  "ds_read_b128 v[40:43], v121 offset:8192\n\t" \
  "ds_read_b128 v[44:47], v121 offset:9216\n\t" \
  "ds_read_b128 v[48:51], v121 offset:10240\n\t" \
  "ds_read_b128 v[52:55], v121 offset:11264\n\t" \
  "ds_read_b128 v[56:59], v121 offset:12288\n\t" \
  "s_waitcnt lgkmcnt(0)\n\t" \
  "s_barrier\n\t" \
  "v_mfma_f32_16x16x32_bf16 v[0:3], v[40:43], v[140:143], 0\n\t" \
  "ds_read_b128 v[60:63], v121 offset:28672\n\t" \
  "v_mfma_f32_16x16x32_bf16 v[20:23], v[40:43], v[144:147], 0\n\t" \
  "ds_read_b128 v[64:67], v121 offset:29696\n\t" \
  "v_mfma_f32_16x16x32_bf16 v[4:7], v[44:47], v[140:143], 0\n\t" \
  "ds_read_b128 v[68:71], v121 offset:30720\n\t" \
  "v_mfma_f32_16x16x32_bf16 v[24:27], v[44:47], v[144:147], 0\n\t" \
  "ds_read_b128 v[72:75], v121 offset:31744\n\t" \
  "v_mfma_f32_16x16x32_bf16 v[8:11], v[48:51], v[140:143], 0\n\t" \
  "ds_read_b128 v[76:79], v121 offset:32768\n\t" \
  "v_mfma_f32_16x16x32_bf16 v[28:31], v[48:51], v[144:147], 0\n\t" \
  "s_add_u32 m0, %[lw], 73728\n\t" \
  "s_nop 0\n\t" \
  "global_load_lds_dwordx4 v236, s[48:49] sc1\n\t" \
  "s_cmp_eq_u32 %[th], 0\n\t" \
  "global_load_lds_dwordx4 v236, s[48:49] offset:1024 sc1\n\t" \
  "s_cbranch_scc0 LD_o1_%=\n\t" \
  "global_load_lds_dwordx4 v236, s[48:49] offset:2048 sc1\n\t" \
  "LD_o1_%=:\n\t" \
  "s_add_u32 s48, s48, 5120\n\t" \
  "s_addc_u32 s49, s49, 0\n\t" \
  "v_mfma_f32_16x16x32_bf16 v[12:15], v[52:55], v[140:143], 0\n\t" \
  "v_mfma_f32_16x16x32_bf16 v[32:35], v[52:55], v[144:147], 0\n\t" \
  "v_mfma_f32_16x16x32_bf16 v[16:19], v[56:59], v[140:143], 0\n\t" \
  "v_mfma_f32_16x16x32_bf16 v[36:39], v[56:59], v[144:147], 0\n\t" \
  "s_waitcnt lgkmcnt(0)\n\t" \
  "s_barrier\n\t" \
  "v_mfma_f32_16x16x32_bf16 v[0:3], v[60:63], v[148:151], v[0:3]\n\t" \
  "ds_read_b128 v[40:43], v121 offset:49152\n\t" \
  "v_mfma_f32_16x16x32_bf16 v[20:23], v[60:63], v[152:155], v[20:23]\n\t" \
  "ds_read_b128 v[44:47], v121 offset:50176\n\t" \
  "v_mfma_f32_16x16x32_bf16 v[4:7], v[64:67], v[148:151], v[4:7]\n\t" \
  "ds_read_b128 v[48:51], v121 offset:51200\n\t" \
  "v_mfma_f32_16x16x32_bf16 v[24:27], v[64:67], v[152:155], v[24:27]\n\t" \
  "ds_read_b128 v[52:55], v121 offset:52224\n\t" \
  "v_mfma_f32_16x16x32_bf16 v[8:11], v[68:71], v[148:151], v[8:11]\n\t" \
  "ds_read_b128 v[56:59], v121 offset:53248\n\t" \
  "v_mfma_f32_16x16x32_bf16 v[28:31], v[68:71], v[152:155], v[28:31]\n\t" \
  "s_add_u32 m0, %[lw], 94208\n\t" \
  "s_nop 0\n\t" \
  "global_load_lds_dwordx4 v236, s[48:49] sc1\n\t" \
  "s_cmp_eq_u32 %[th], 0\n\t" \
  "global_load_lds_dwordx4 v236, s[48:49] offset:1024 sc1\n\t" \
  "s_cbranch_scc0 LD_o2_%=\n\t" \
  "global_load_lds_dwordx4 v236, s[48:49] offset:2048 sc1\n\t" \
  "LD_o2_%=:\n\t" \
  "s_add_u32 s48, s48, 5120\n\t" \
  "s_addc_u32 s49, s49, 0\n\t" \
  "v_mfma_f32_16x16x32_bf16 v[12:15], v[72:75], v[148:151], v[12:15]\n\t" \
  "v_mfma_f32_16x16x32_bf16 v[32:35], v[72:75], v[152:155], v[32:35]\n\t" \
  "v_mfma_f32_16x16x32_bf16 v[16:19], v[76:79], v[148:151], v[16:19]\n\t" \
  "v_mfma_f32_16x16x32_bf16 v[36:39], v[76:79], v[152:155], v[36:39]\n\t" \
  "s_waitcnt lgkmcnt(0)\n\t" \
  "s_barrier\n\t" \
  "v_mfma_f32_16x16x32_bf16 v[0:3], v[40:43], v[156:159], v[0:3]\n\t" \
  "ds_read_b128 v[60:63], v122 offset:4096\n\t" \
  "v_mfma_f32_16x16x32_bf16 v[20:23], v[40:43], v[160:163], v[20:23]\n\t" \
  "ds_read_b128 v[64:67], v122 offset:5120\n\t" \
  "v_mfma_f32_16x16x32_bf16 v[4:7], v[44:47], v[156:159], v[4:7]\n\t" \
  "ds_read_b128 v[68:71], v122 offset:6144\n\t" \
  "v_mfma_f32_16x16x32_bf16 v[24:27], v[44:47], v[160:163], v[24:27]\n\t" \
  "ds_read_b128 v[72:75], v122 offset:7168\n\t" \
  "v_mfma_f32_16x16x32_bf16 v[8:11], v[48:51], v[156:159], v[8:11]\n\t" \
  "ds_read_b128 v[76:79], v122 offset:8192\n\t" \
  "v_mfma_f32_16x16x32_bf16 v[28:31], v[48:51], v[160:163], v[28:31]\n\t" \
  "s_add_u32 m0, %[lw], 114688\n\t" \
  "s_nop 0\n\t" \
  "global_load_lds_dwordx4 v236, s[48:49] sc1\n\t" \
  "s_cmp_eq_u32 %[th], 0\n\t" \
  "global_load_lds_dwordx4 v236, s[48:49] offset:1024 sc1\n\t" \
  "s_cbranch_scc0 LD_o3_%=\n\t" \
  "global_load_lds_dwordx4 v236, s[48:49] offset:2048 sc1\n\t" \
  "LD_o3_%=:\n\t" \
  "s_add_u32 s48, s48, 5120\n\t" \
  "s_addc_u32 s49, s49, 0\n\t" \
  "v_mfma_f32_16x16x32_bf16 v[12:15], v[52:55], v[156:159], v[12:15]\n\t" \
  "v_mfma_f32_16x16x32_bf16 v[32:35], v[52:55], v[160:163], v[32:35]\n\t" \
  "v_mfma_f32_16x16x32_bf16 v[16:19], v[56:59], v[156:159], v[16:19]\n\t" \
  "v_mfma_f32_16x16x32_bf16 v[36:39], v[56:59], v[160:163], v[36:39]\n\t" \
  "s_waitcnt vmcnt(6)\n\t" \
  "s_mov_b64 s[44:45], %[tag]\n\t" \
  "s_mov_b64 exec, 1\n\t" \
  "v_mov_b32 v124, s44\n\t" \
  "v_mov_b32 v125, s45\n\t" \
  "global_store_dwordx2 v128, v[124:125], %[wsb] sc1\n\t" \
  "s_mov_b64 exec, -1\n\t" \
  "s_waitcnt lgkmcnt(0)\n\t" \
  "s_barrier\n\t" \
  "v_mfma_f32_16x16x32_bf16 v[0:3], v[60:63], v[164:167], v[0:3]\n\t" \
  "ds_read_b128 v[40:43], v120 offset:0\n\t" \
  "v_mfma_f32_16x16x32_bf16 v[20:23], v[60:63], v[168:171], v[20:23]\n\t" \
  "ds_read_b128 v[44:47], v120 offset:1024\n\t" \
  "v_mfma_f32_16x16x32_bf16 v[4:7], v[64:67], v[164:167], v[4:7]\n\t" \
  "ds_read_b128 v[48:51], v120 offset:2048\n\t" \
  "v_mfma_f32_16x16x32_bf16 v[24:27], v[64:67], v[168:171], v[24:27]\n\t" \
  "ds_read_b128 v[52:55], v120 offset:3072\n\t" \
  "v_mfma_f32_16x16x32_bf16 v[8:11], v[68:71], v[164:167], v[8:11]\n\t" \
  "ds_read_b128 v[56:59], v120 offset:4096\n\t" \
  "v_mfma_f32_16x16x32_bf16 v[28:31], v[68:71], v[168:171], v[28:31]\n\t" \
  "s_add_u32 m0, %[lw], 135168\n\t" \
  "s_nop 0\n\t" \
  "global_load_lds_dwordx4 v236, s[48:49] sc1\n\t" \
  "s_cmp_eq_u32 %[th], 0\n\t" \
  "global_load_lds_dwordx4 v236, s[48:49] offset:1024 sc1\n\t" \
  "s_cbranch_scc0 LD_o4_%=\n\t" \
  "global_load_lds_dwordx4 v236, s[48:49] offset:2048 sc1\n\t" \
  "LD_o4_%=:\n\t" \
  "s_add_u32 s48, s48, 5120\n\t" \
  "s_addc_u32 s49, s49, 0\n\t" \
  "v_mfma_f32_16x16x32_bf16 v[12:15], v[72:75], v[164:167], v[12:15]\n\t" \
  "v_mfma_f32_16x16x32_bf16 v[32:35], v[72:75], v[168:171], v[32:35]\n\t" \
  "v_mfma_f32_16x16x32_bf16 v[16:19], v[76:79], v[164:167], v[16:19]\n\t" \
  "v_mfma_f32_16x16x32_bf16 v[36:39], v[76:79], v[168:171], v[36:39]\n\t" \
  "s_waitcnt lgkmcnt(0)\n\t" \
  "s_barrier\n\t" \
  "v_mfma_f32_16x16x32_bf16 v[0:3], v[40:43], v[172:175], v[0:3]\n\t" \
  "ds_read_b128 v[60:63], v120 offset:20480\n\t" \
  "v_mfma_f32_16x16x32_bf16 v[20:23], v[40:43], v[176:179], v[20:23]\n\t" \
  "ds_read_b128 v[64:67], v120 offset:21504\n\t" \
  "v_mfma_f32_16x16x32_bf16 v[4:7], v[44:47], v[172:175], v[4:7]\n\t" \
  "ds_read_b128 v[68:71], v120 offset:22528\n\t" \
  "v_mfma_f32_16x16x32_bf16 v[24:27], v[44:47], v[176:179], v[24:27]\n\t" \
  "ds_read_b128 v[72:75], v120 offset:23552\n\t" \
  "v_mfma_f32_16x16x32_bf16 v[8:11], v[48:51], v[172:175], v[8:11]\n\t" \
  "ds_read_b128 v[76:79], v120 offset:24576\n\t" \
  "v_mfma_f32_16x16x32_bf16 v[28:31], v[48:51], v[176:179], v[28:31]\n\t" \
  "s_add_u32 m0, %[lw], 0\n\t" \
  "s_nop 0\n\t" \
  "global_load_lds_dwordx4 v236, s[48:49] sc1\n\t" \
  "s_cmp_eq_u32 %[th], 0\n\t" \
  "global_load_lds_dwordx4 v236, s[48:49] offset:1024 sc1\n\t" \
  "s_cbranch_scc0 LD_o5_%=\n\t" \
  "global_load_lds_dwordx4 v236, s[48:49] offset:2048 sc1\n\t" \
  "LD_o5_%=:\n\t" \
  "s_add_u32 s48, s48, 5120\n\t" \
  "s_addc_u32 s49, s49, 0\n\t" \
  "v_mfma_f32_16x16x32_bf16 v[12:15], v[52:55], v[172:175], v[12:15]\n\t" \
  "v_mfma_f32_16x16x32_bf16 v[32:35], v[52:55], v[176:179], v[32:35]\n\t" \
  "v_mfma_f32_16x16x32_bf16 v[16:19], v[56:59], v[172:175], v[16:19]\n\t" \
  "v_mfma_f32_16x16x32_bf16 v[36:39], v[56:59], v[176:179], v[36:39]\n\t" \
  "s_waitcnt lgkmcnt(0)\n\t" \
  "s_barrier\n\t" \
  "v_mfma_f32_16x16x32_bf16 v[0:3], v[60:63], v[180:183], v[0:3]\n\t" \
  "ds_read_b128 v[40:43], v120 offset:40960\n\t" \
  "v_mfma_f32_16x16x32_bf16 v[20:23], v[60:63], v[184:187], v[20:23]\n\t" \
  "ds_read_b128 v[44:47], v120 offset:41984\n\t" \
  "v_mfma_f32_16x16x32_bf16 v[4:7], v[64:67], v[180:183], v[4:7]\n\t" \
  "ds_read_b128 v[48:51], v120 offset:43008\n\t" \
  "v_mfma_f32_16x16x32_bf16 v[24:27], v[64:67], v[184:187], v[24:27]\n\t" \
  "ds_read_b128 v[52:55], v120 offset:44032\n\t" \
  "v_mfma_f32_16x16x32_bf16 v[8:11], v[68:71], v[180:183], v[8:11]\n\t" \
  "ds_read_b128 v[56:59], v120 offset:45056\n\t" \
  "v_mfma_f32_16x16x32_bf16 v[28:31], v[68:71], v[184:187], v[28:31]\n\t" \
  "s_add_u32 m0, %[lw], 20480\n\t" \
  "s_mov_b64 s[48:49], %[gwo]\n\t" \
  "global_load_lds_dwordx4 v236, s[48:49] sc1\n\t" \
  "s_cmp_eq_u32 %[th], 0\n\t" \
  "global_load_lds_dwordx4 v236, s[48:49] offset:1024 sc1\n\t" \
  "s_cbranch_scc0 LD_o6_%=\n\t" \
  "global_load_lds_dwordx4 v236, s[48:49] offset:2048 sc1\n\t" \
  "LD_o6_%=:\n\t" \
  "s_add_u32 s48, s48, 5120\n\t" \
  "s_addc_u32 s49, s49, 0\n\t" \
  "v_mfma_f32_16x16x32_bf16 v[12:15], v[72:75], v[180:183], v[12:15]\n\t" \
  "v_mfma_f32_16x16x32_bf16 v[32:35], v[72:75], v[184:187], v[32:35]\n\t" \
  "v_mfma_f32_16x16x32_bf16 v[16:19], v[76:79], v[180:183], v[16:19]\n\t" \
  "v_mfma_f32_16x16x32_bf16 v[36:39], v[76:79], v[184:187], v[36:39]\n\t" \
  "s_waitcnt vmcnt(11) lgkmcnt(0)\n\t" \
  "s_barrier\n\t" \
  "v_mfma_f32_16x16x32_bf16 v[0:3], v[40:43], v[188:191], v[0:3]\n\t" \
  "ds_read_b128 v[60:63], v121 offset:8192\n\t" \
  "v_mfma_f32_16x16x32_bf16 v[20:23], v[40:43], v[192:195], v[20:23]\n\t" \
  "ds_read_b128 v[64:67], v121 offset:9216\n\t" \
  "v_mfma_f32_16x16x32_bf16 v[4:7], v[44:47], v[188:191], v[4:7]\n\t" \
  "ds_read_b128 v[68:71], v121 offset:10240\n\t" \
  "v_mfma_f32_16x16x32_bf16 v[24:27], v[44:47], v[192:195], v[24:27]\n\t" \
  "ds_read_b128 v[72:75], v121 offset:11264\n\t" \
  "v_mfma_f32_16x16x32_bf16 v[8:11], v[48:51], v[188:191], v[8:11]\n\t" \
  "ds_read_b128 v[76:79], v121 offset:12288\n\t" \
  "v_mfma_f32_16x16x32_bf16 v[28:31], v[48:51], v[192:195], v[28:31]\n\t" \
  "s_add_u32 m0, %[lw], 40960\n\t" \
  "s_nop 0\n\t" \
  "global_load_lds_dwordx4 v236, s[48:49] sc1\n\t" \
  "s_cmp_eq_u32 %[th], 0\n\t" \
  "global_load_lds_dwordx4 v236, s[48:49] offset:1024 sc1\n\t" \
  "s_cbranch_scc0 LD_o7_%=\n\t" \
  "global_load_lds_dwordx4 v236, s[48:49] offset:2048 sc1\n\t" \
  "LD_o7_%=:\n\t" \
  "s_add_u32 s48, s48, 5120\n\t" \
  "s_addc_u32 s49, s49, 0\n\t" \
  "v_mfma_f32_16x16x32_bf16 v[12:15], v[52:55], v[188:191], v[12:15]\n\t" \
  "v_mfma_f32_16x16x32_bf16 v[32:35], v[52:55], v[192:195], v[32:35]\n\t" \
  "v_mfma_f32_16x16x32_bf16 v[16:19], v[56:59], v[188:191], v[16:19]\n\t" \
  "v_mfma_f32_16x16x32_bf16 v[36:39], v[56:59], v[192:195], v[36:39]\n\t" \
  "s_waitcnt vmcnt(11) lgkmcnt(0)\n\t" \
  "s_barrier\n\t" \
  "v_mfma_f32_16x16x32_bf16 v[0:3], v[60:63], v[196:199], v[0:3]\n\t" \
  "ds_read_b128 v[40:43], v121 offset:28672\n\t" \
  "v_mfma_f32_16x16x32_bf16 v[20:23], v[60:63], v[200:203], v[20:23]\n\t" \
  "ds_read_b128 v[44:47], v121 offset:29696\n\t" \
  "v_mfma_f32_16x16x32_bf16 v[4:7], v[64:67], v[196:199], v[4:7]\n\t" \
  "ds_read_b128 v[48:51], v121 offset:30720\n\t" \
  "v_mfma_f32_16x16x32_bf16 v[24:27], v[64:67], v[200:203], v[24:27]\n\t" \
  "ds_read_b128 v[52:55], v121 offset:31744\n\t" \
  "v_mfma_f32_16x16x32_bf16 v[8:11], v[68:71], v[196:199], v[8:11]\n\t" \
  "ds_read_b128 v[56:59], v121 offset:32768\n\t" \
  "v_mfma_f32_16x16x32_bf16 v[28:31], v[68:71], v[200:203], v[28:31]\n\t" \
  "s_add_u32 m0, %[lw], 73728\n\t" \
  "s_nop 0\n\t" \
  "global_load_lds_dwordx4 v236, s[48:49] sc1\n\t" \
  "s_cmp_eq_u32 %[th], 0\n\t" \
  "global_load_lds_dwordx4 v236, s[48:49] offset:1024 sc1\n\t" \
  "s_cbranch_scc0 LD_o8_%=\n\t" \
  "global_load_lds_dwordx4 v236, s[48:49] offset:2048 sc1\n\t" \
  "LD_o8_%=:\n\t" \
  "s_add_u32 s48, s48, 5120\n\t" \
  "s_addc_u32 s49, s49, 0\n\t" \
  "v_mfma_f32_16x16x32_bf16 v[12:15], v[72:75], v[196:199], v[12:15]\n\t" \
  "v_mfma_f32_16x16x32_bf16 v[32:35], v[72:75], v[200:203], v[32:35]\n\t" \
  "v_mfma_f32_16x16x32_bf16 v[16:19], v[76:79], v[196:199], v[16:19]\n\t" \
  "v_mfma_f32_16x16x32_bf16 v[36:39], v[76:79], v[200:203], v[36:39]\n\t" \
  "global_load_dwordx2 v[124:125], v123, %[wsb] sc1\n\t" \
  "s_waitcnt vmcnt(12) lgkmcnt(0)\n\t" \
  "s_barrier\n\t" \
  "v_mfma_f32_16x16x32_bf16 v[0:3], v[40:43], v[204:207], v[0:3]\n\t" \
  "ds_read_b128 v[60:63], v121 offset:49152\n\t" \
  "v_mfma_f32_16x16x32_bf16 v[20:23], v[40:43], v[208:211], v[20:23]\n\t" \
  "ds_read_b128 v[64:67], v121 offset:50176\n\t" \
  "v_mfma_f32_16x16x32_bf16 v[4:7], v[44:47], v[204:207], v[4:7]\n\t" \
  "ds_read_b128 v[68:71], v121 offset:51200\n\t" \
  "v_mfma_f32_16x16x32_bf16 v[24:27], v[44:47], v[208:211], v[24:27]\n\t" \
  "ds_read_b128 v[72:75], v121 offset:52224\n\t" \
  "v_mfma_f32_16x16x32_bf16 v[8:11], v[48:51], v[204:207], v[8:11]\n\t" \
  "ds_read_b128 v[76:79], v121 offset:53248\n\t" \
  "v_mfma_f32_16x16x32_bf16 v[28:31], v[48:51], v[208:211], v[28:31]\n\t" \
  "s_add_u32 m0, %[lw], 94208\n\t" \
  "s_nop 0\n\t" \
  "global_load_lds_dwordx4 v236, s[48:49] sc1\n\t" \
  "s_cmp_eq_u32 %[th], 0\n\t" \
  "global_load_lds_dwordx4 v236, s[48:49] offset:1024 sc1\n\t" \
  "s_cbranch_scc0 LD_o9_%=\n\t" \
  "global_load_lds_dwordx4 v236, s[48:49] offset:2048 sc1\n\t" \
  "LD_o9_%=:\n\t" \
  "s_add_u32 s48, s48, 5120\n\t" \
  "s_addc_u32 s49, s49, 0\n\t" \
  "v_mfma_f32_16x16x32_bf16 v[12:15], v[52:55], v[204:207], v[12:15]\n\t" \
  "v_mfma_f32_16x16x32_bf16 v[32:35], v[52:55], v[208:211], v[32:35]\n\t" \
  "v_mfma_f32_16x16x32_bf16 v[16:19], v[56:59], v[204:207], v[16:19]\n\t" \
  "v_mfma_f32_16x16x32_bf16 v[36:39], v[56:59], v[208:211], v[36:39]\n\t" \
  "s_waitcnt vmcnt(11) lgkmcnt(0)\n\t" \
  "s_barrier\n\t" \
  "v_mfma_f32_16x16x32_bf16 v[0:3], v[60:63], v[212:215], v[0:3]\n\t" \
  "ds_read_b128 v[40:43], v122 offset:4096\n\t" \
  "v_mfma_f32_16x16x32_bf16 v[20:23], v[60:63], v[216:219], v[20:23]\n\t" \
  "ds_read_b128 v[44:47], v122 offset:5120\n\t" \
  "v_mfma_f32_16x16x32_bf16 v[4:7], v[64:67], v[212:215], v[4:7]\n\t" \
  "ds_read_b128 v[48:51], v122 offset:6144\n\t" \
  "v_mfma_f32_16x16x32_bf16 v[24:27], v[64:67], v[216:219], v[24:27]\n\t" \
  "ds_read_b128 v[52:55], v122 offset:7168\n\t" \
  "v_mfma_f32_16x16x32_bf16 v[8:11], v[68:71], v[212:215], v[8:11]\n\t" \
  "ds_read_b128 v[56:59], v122 offset:8192\n\t" \
  "v_mfma_f32_16x16x32_bf16 v[28:31], v[68:71], v[216:219], v[28:31]\n\t" \
  "s_add_u32 m0, %[lw], 114688\n\t" \
  "s_nop 0\n\t" \
  "global_load_lds_dwordx4 v236, s[48:49] sc1\n\t" \
  "s_cmp_eq_u32 %[th], 0\n\t" \
  "global_load_lds_dwordx4 v236, s[48:49] offset:1024 sc1\n\t" \
  "s_cbranch_scc0 LD_o10_%=\n\t" \
  "global_load_lds_dwordx4 v236, s[48:49] offset:2048 sc1\n\t" \
  "LD_o10_%=:\n\t" \
  "s_add_u32 s48, s48, 5120\n\t" \
  "s_addc_u32 s49, s49, 0\n\t" \
  "v_mfma_f32_16x16x32_bf16 v[12:15], v[72:75], v[212:215], v[12:15]\n\t" \
  "v_mfma_f32_16x16x32_bf16 v[32:35], v[72:75], v[216:219], v[32:35]\n\t" \
  "v_mfma_f32_16x16x32_bf16 v[16:19], v[76:79], v[212:215], v[16:19]\n\t" \
  "v_mfma_f32_16x16x32_bf16 v[36:39], v[76:79], v[216:219], v[36:39]\n\t" \
  "s_waitcnt vmcnt(4)\n\t" \
  "v_cmp_ne_u64 vcc, %[tag], v[124:125]\n\t" \
  "s_waitcnt lgkmcnt(0)\n\t" \
  "s_barrier\n\t" \
  "v_mfma_f32_16x16x32_bf16 v[0:3], v[40:43], v[220:223], v[0:3]\n\t" \
  "ds_read_b128 v[60:63], v120 offset:0\n\t" \
  "v_mfma_f32_16x16x32_bf16 v[20:23], v[40:43], v[224:227], v[20:23]\n\t" \
  "ds_read_b128 v[64:67], v120 offset:1024\n\t" \
  "v_mfma_f32_16x16x32_bf16 v[4:7], v[44:47], v[220:223], v[4:7]\n\t" \
  "ds_read_b128 v[68:71], v120 offset:2048\n\t" \
  "v_mfma_f32_16x16x32_bf16 v[24:27], v[44:47], v[224:227], v[24:27]\n\t" \
  "ds_read_b128 v[72:75], v120 offset:3072\n\t" \
  "v_mfma_f32_16x16x32_bf16 v[8:11], v[48:51], v[220:223], v[8:11]\n\t" \
  "ds_read_b128 v[76:79], v120 offset:4096\n\t" \
  "v_mfma_f32_16x16x32_bf16 v[28:31], v[48:51], v[224:227], v[28:31]\n\t" \
  "s_cbranch_vccnz LD_o11_%=\n\t" \
  "global_load_dwordx4 v[80:83], v236, %[ga0] offset:0 sc1\n\t" \
  "global_load_dwordx4 v[84:87], v236, %[ga0] offset:1024 sc1\n\t" \
  "global_load_dwordx4 v[88:91], v236, %[ga1] offset:0 sc1\n\t" \
  "global_load_dwordx4 v[92:95], v236, %[ga1] offset:1024 sc1\n\t" \
  "global_load_dwordx4 v[96:99], v236, %[ga2] offset:0 sc1\n\t" \
  "LD_o11_%=:\n\t" \
  "v_mfma_f32_16x16x32_bf16 v[12:15], v[52:55], v[220:223], v[12:15]\n\t" \
  "v_mfma_f32_16x16x32_bf16 v[32:35], v[52:55], v[224:227], v[32:35]\n\t" \
  "v_mfma_f32_16x16x32_bf16 v[16:19], v[56:59], v[220:223], v[16:19]\n\t" \
  "v_mfma_f32_16x16x32_bf16 v[36:39], v[56:59], v[224:227], v[36:39]\n\t" \
  "s_waitcnt lgkmcnt(0)\n\t" \
  "s_barrier\n\t" \
  "v_mfma_f32_16x16x32_bf16 v[0:3], v[60:63], v[228:231], v[0:3]\n\t" \
  "ds_read_b128 v[40:43], v120 offset:20480\n\t" \
  "v_mfma_f32_16x16x32_bf16 v[20:23], v[60:63], v[232:235], v[20:23]\n\t" \
  "ds_read_b128 v[44:47], v120 offset:21504\n\t" \
  "v_mfma_f32_16x16x32_bf16 v[4:7], v[64:67], v[228:231], v[4:7]\n\t" \
  "ds_read_b128 v[48:51], v120 offset:22528\n\t" \
  "v_mfma_f32_16x16x32_bf16 v[24:27], v[64:67], v[232:235], v[24:27]\n\t" \
  "ds_read_b128 v[52:55], v120 offset:23552\n\t" \
  "v_mfma_f32_16x16x32_bf16 v[8:11], v[68:71], v[228:231], v[8:11]\n\t" \
  "ds_read_b128 v[56:59], v120 offset:24576\n\t" \
  "v_mfma_f32_16x16x32_bf16 v[28:31], v[68:71], v[232:235], v[28:31]\n\t" \
  "s_cbranch_vccnz LD_o12_%=\n\t" \
  "global_load_dwordx4 v[100:103], v236, %[ga2] offset:1024 sc1\n\t" \
  "global_load_dwordx4 v[104:107], v236, %[ga3] offset:0 sc1\n\t" \
  "global_load_dwordx4 v[108:111], v236, %[ga3] offset:1024 sc1\n\t" \
  "global_load_dwordx4 v[112:115], v236, %[ga4] offset:0 sc1\n\t" \
  "global_load_dwordx4 v[116:119], v236, %[ga4] offset:1024 sc1\n\t" \
  "LD_o12_%=:\n\t" \
  "v_mfma_f32_16x16x32_bf16 v[12:15], v[72:75], v[228:231], v[12:15]\n\t" \
  "v_mfma_f32_16x16x32_bf16 v[32:35], v[72:75], v[232:235], v[32:35]\n\t" \
  "v_mfma_f32_16x16x32_bf16 v[16:19], v[76:79], v[228:231], v[16:19]\n\t" \
  "v_mfma_f32_16x16x32_bf16 v[36:39], v[76:79], v[232:235], v[36:39]\n\t" \
  "s_cbranch_vccz LD_o13_%=\n\t" \
  "s_waitcnt vmcnt(0)\n\t" \
  "LD_o13_%=:\n\t" \
  "s_waitcnt vmcnt(10)\n\t" \
  "s_nop 3\n\t" \
  "s_waitcnt lgkmcnt(0)\n\t" \
  "s_barrier\n\t" \
  "s_cbranch_vccz LD_have_%=\n\t" \
  "s_mov_b32 s45, 0x400000\n\t" \
  "LD_spin_%=:\n\t" \
  "global_load_dwordx2 v[124:125], v123, %[wsb] sc1\n\t" \
  "s_sub_u32 s45, s45, 1\n\t" \
  "s_cbranch_scc1 LD_ld_%=\n\t" \
  "s_waitcnt vmcnt(0)\n\t" \
  "v_cmp_ne_u64 vcc, %[tag], v[124:125]\n\t" \
  "s_nop 4\n\t" \
  "s_cbranch_vccz LD_ld_%=\n\t" \
  "s_sleep 2\n\t" \
  "s_branch LD_spin_%=\n\t" \
  "LD_ld_%=:\n\t" \
  "global_load_dwordx4 v[80:83], v236, %[ga0] offset:0 sc1\n\t" \
  "global_load_dwordx4 v[84:87], v236, %[ga0] offset:1024 sc1\n\t" \
  "global_load_dwordx4 v[88:91], v236, %[ga1] offset:0 sc1\n\t" \
  "global_load_dwordx4 v[92:95], v236, %[ga1] offset:1024 sc1\n\t" \
  "global_load_dwordx4 v[96:99], v236, %[ga2] offset:0 sc1\n\t" \
  "global_load_dwordx4 v[100:103], v236, %[ga2] offset:1024 sc1\n\t" \
  "global_load_dwordx4 v[104:107], v236, %[ga3] offset:0 sc1\n\t" \
  "global_load_dwordx4 v[108:111], v236, %[ga3] offset:1024 sc1\n\t" \
  "global_load_dwordx4 v[112:115], v236, %[ga4] offset:0 sc1\n\t" \
  "global_load_dwordx4 v[116:119], v236, %[ga4] offset:1024 sc1\n\t" \
  "LD_have_%=:\n\t" \
  "v_mul_f32 v0, v126, v0\n\t" \
  "v_mul_f32 v1, v126, v1\n\t" \
  "v_mul_f32 v2, v126, v2\n\t" \
  "v_mul_f32 v3, v126, v3\n\t" \
  "ds_read_b128 v[60:63], v120 offset:40960\n\t" \
  "ds_read_b128 v[64:67], v120 offset:41984\n\t" \
  "ds_read_b128 v[68:71], v120 offset:43008\n\t" \
  "ds_read_b128 v[72:75], v120 offset:44032\n\t" \
  "ds_read_b128 v[76:79], v120 offset:45056\n\t" \
  "s_waitcnt vmcnt(8)\n\t" \
  "v_mul_f32 v20, v127, v20\n\t" \
  "v_mul_f32 v21, v127, v21\n\t" \
  "v_mul_f32 v22, v127, v22\n\t" \
  "v_mul_f32 v23, v127, v23\n\t" \
  "v_mfma_f32_16x16x32_bf16 v[0:3], v[40:43], v[80:83], v[0:3]\n\t" \
  "v_mul_f32 v4, v126, v4\n\t" \
  "v_mul_f32 v5, v126, v5\n\t" \
  "v_mul_f32 v6, v126, v6\n\t" \
  "v_mul_f32 v7, v126, v7\n\t" \
  "v_mfma_f32_16x16x32_bf16 v[20:23], v[40:43], v[84:87], v[20:23]\n\t" \
  "v_mul_f32 v24, v127, v24\n\t" \
  "v_mul_f32 v25, v127, v25\n\t" \
  "v_mul_f32 v26, v127, v26\n\t" \
  "v_mul_f32 v27, v127, v27\n\t" \
  "v_mfma_f32_16x16x32_bf16 v[4:7], v[44:47], v[80:83], v[4:7]\n\t" \
  "v_mul_f32 v8, v126, v8\n\t" \
  "v_mul_f32 v9, v126, v9\n\t" \
  "v_mul_f32 v10, v126, v10\n\t" \
  "v_mul_f32 v11, v126, v11\n\t" \
  "v_mfma_f32_16x16x32_bf16 v[24:27], v[44:47], v[84:87], v[24:27]\n\t" \
  "v_mul_f32 v28, v127, v28\n\t" \
  "v_mul_f32 v29, v127, v29\n\t" \
  "v_mul_f32 v30, v127, v30\n\t" \
  "v_mul_f32 v31, v127, v31\n\t" \
  "v_mfma_f32_16x16x32_bf16 v[8:11], v[48:51], v[80:83], v[8:11]\n\t" \
  "v_mul_f32 v12, v126, v12\n\t" \
  "v_mul_f32 v13, v126, v13\n\t" \
  "v_mul_f32 v14, v126, v14\n\t" \
  "v_mul_f32 v15, v126, v15\n\t" \
  "v_mfma_f32_16x16x32_bf16 v[28:31], v[48:51], v[84:87], v[28:31]\n\t" \
  "v_mul_f32 v32, v127, v32\n\t" \
  "v_mul_f32 v33, v127, v33\n\t" \
  "v_mul_f32 v34, v127, v34\n\t" \
  "v_mul_f32 v35, v127, v35\n\t" \
  "v_mfma_f32_16x16x32_bf16 v[12:15], v[52:55], v[80:83], v[12:15]\n\t" \
  "v_mul_f32 v16, v126, v16\n\t" \
  "v_mul_f32 v17, v126, v17\n\t" \
  "v_mul_f32 v18, v126, v18\n\t" \
  "v_mul_f32 v19, v126, v19\n\t" \
  "v_mfma_f32_16x16x32_bf16 v[32:35], v[52:55], v[84:87], v[32:35]\n\t" \
  "v_mul_f32 v36, v127, v36\n\t" \
  "v_mul_f32 v37, v127, v37\n\t" \
  "v_mul_f32 v38, v127, v38\n\t" \
  "v_mul_f32 v39, v127, v39\n\t" \
  "v_mfma_f32_16x16x32_bf16 v[16:19], v[56:59], v[80:83], v[16:19]\n\t" \
  "s_nop 0\n\t" \
  "v_mfma_f32_16x16x32_bf16 v[36:39], v[56:59], v[84:87], v[36:39]\n\t" \
  "s_waitcnt lgkmcnt(0)\n\t" \
  "ds_read_b128 v[40:43], v121 offset:8192\n\t" \
  "ds_read_b128 v[44:47], v121 offset:9216\n\t" \
  "ds_read_b128 v[48:51], v121 offset:10240\n\t" \
  "ds_read_b128 v[52:55], v121 offset:11264\n\t" \
  "ds_read_b128 v[56:59], v121 offset:12288\n\t" \
  "s_waitcnt vmcnt(6)\n\t" \
  "v_mfma_f32_16x16x32_bf16 v[0:3], v[60:63], v[88:91], v[0:3]\n\t" \
  "v_mfma_f32_16x16x32_bf16 v[20:23], v[60:63], v[92:95], v[20:23]\n\t" \
  "v_mfma_f32_16x16x32_bf16 v[4:7], v[64:67], v[88:91], v[4:7]\n\t" \
  "v_mfma_f32_16x16x32_bf16 v[24:27], v[64:67], v[92:95], v[24:27]\n\t" \
  "v_mfma_f32_16x16x32_bf16 v[8:11], v[68:71], v[88:91], v[8:11]\n\t" \
  "v_mfma_f32_16x16x32_bf16 v[28:31], v[68:71], v[92:95], v[28:31]\n\t" \
  "v_mfma_f32_16x16x32_bf16 v[12:15], v[72:75], v[88:91], v[12:15]\n\t" \
  "v_mfma_f32_16x16x32_bf16 v[32:35], v[72:75], v[92:95], v[32:35]\n\t" \
  "v_mfma_f32_16x16x32_bf16 v[16:19], v[76:79], v[88:91], v[16:19]\n\t" \
  "v_mfma_f32_16x16x32_bf16 v[36:39], v[76:79], v[92:95], v[36:39]\n\t" \
  "s_waitcnt lgkmcnt(0)\n\t" \
  "s_barrier\n\t" \
  "ds_read_b128 v[60:63], v121 offset:28672\n\t" \
  "ds_read_b128 v[64:67], v121 offset:29696\n\t" \
  "ds_read_b128 v[68:71], v121 offset:30720\n\t" \
  "ds_read_b128 v[72:75], v121 offset:31744\n\t" \
  "ds_read_b128 v[76:79], v121 offset:32768\n\t" \
  "s_waitcnt vmcnt(4)\n\t" \
  "v_mfma_f32_16x16x32_bf16 v[0:3], v[40:43], v[96:99], v[0:3]\n\t" \
  "v_mfma_f32_16x16x32_bf16 v[20:23], v[40:43], v[100:103], v[20:23]\n\t" \
  "v_mfma_f32_16x16x32_bf16 v[4:7], v[44:47], v[96:99], v[4:7]\n\t" \
  "v_mfma_f32_16x16x32_bf16 v[24:27], v[44:47], v[100:103], v[24:27]\n\t" \
  "v_mfma_f32_16x16x32_bf16 v[8:11], v[48:51], v[96:99], v[8:11]\n\t" \
  "v_mfma_f32_16x16x32_bf16 v[28:31], v[48:51], v[100:103], v[28:31]\n\t" \
  "v_mfma_f32_16x16x32_bf16 v[12:15], v[52:55], v[96:99], v[12:15]\n\t" \
  "v_mfma_f32_16x16x32_bf16 v[32:35], v[52:55], v[100:103], v[32:35]\n\t" \
  "v_mfma_f32_16x16x32_bf16 v[16:19], v[56:59], v[96:99], v[16:19]\n\t" \
  "v_mfma_f32_16x16x32_bf16 v[36:39], v[56:59], v[100:103], v[36:39]\n\t" \
  "s_waitcnt lgkmcnt(0)\n\t" \
  "ds_read_b128 v[40:43], v121 offset:49152\n\t" \
  "ds_read_b128 v[44:47], v121 offset:50176\n\t" \
  "ds_read_b128 v[48:51], v121 offset:51200\n\t" \
  "ds_read_b128 v[52:55], v121 offset:52224\n\t" \
  "ds_read_b128 v[56:59], v121 offset:53248\n\t" \
  "s_waitcnt vmcnt(2)\n\t" \
  "v_mfma_f32_16x16x32_bf16 v[0:3], v[60:63], v[104:107], v[0:3]\n\t" \
  "v_mfma_f32_16x16x32_bf16 v[20:23], v[60:63], v[108:111], v[20:23]\n\t" \
  "v_mfma_f32_16x16x32_bf16 v[4:7], v[64:67], v[104:107], v[4:7]\n\t" \
  "v_mfma_f32_16x16x32_bf16 v[24:27], v[64:67], v[108:111], v[24:27]\n\t" \
  "v_mfma_f32_16x16x32_bf16 v[8:11], v[68:71], v[104:107], v[8:11]\n\t" \
  "v_mfma_f32_16x16x32_bf16 v[28:31], v[68:71], v[108:111], v[28:31]\n\t" \
  "v_mfma_f32_16x16x32_bf16 v[12:15], v[72:75], v[104:107], v[12:15]\n\t" \
  "v_mfma_f32_16x16x32_bf16 v[32:35], v[72:75], v[108:111], v[32:35]\n\t" \
  "v_mfma_f32_16x16x32_bf16 v[16:19], v[76:79], v[104:107], v[16:19]\n\t" \
  "v_mfma_f32_16x16x32_bf16 v[36:39], v[76:79], v[108:111], v[36:39]\n\t" \
  "s_waitcnt lgkmcnt(0)\n\t" \
  "s_waitcnt vmcnt(0)\n\t" \
  "v_mfma_f32_16x16x32_bf16 v[0:3], v[40:43], v[112:115], v[0:3]\n\t" \
  "v_mfma_f32_16x16x32_bf16 v[4:7], v[44:47], v[112:115], v[4:7]\n\t" \
  "v_mfma_f32_16x16x32_bf16 v[8:11], v[48:51], v[112:115], v[8:11]\n\t" \
  "v_mfma_f32_16x16x32_bf16 v[12:15], v[52:55], v[112:115], v[12:15]\n\t" \
  "v_mfma_f32_16x16x32_bf16 v[16:19], v[56:59], v[112:115], v[16:19]\n\t" \
  "v_mfma_f32_16x16x32_bf16 v[20:23], v[40:43], v[116:119], v[20:23]\n\t" \
  "ds_write_b128 v129, v[0:3] offset:0\n\t" \
  "v_mfma_f32_16x16x32_bf16 v[24:27], v[44:47], v[116:119], v[24:27]\n\t" \
  "ds_write_b128 v129, v[4:7] offset:1024\n\t" \
  "v_mfma_f32_16x16x32_bf16 v[28:31], v[48:51], v[116:119], v[28:31]\n\t" \
  "ds_write_b128 v129, v[8:11] offset:2048\n\t" \
  "v_mfma_f32_16x16x32_bf16 v[32:35], v[52:55], v[116:119], v[32:35]\n\t" \
  "ds_write_b128 v129, v[12:15] offset:3072\n\t" \
  "v_mfma_f32_16x16x32_bf16 v[36:39], v[56:59], v[116:119], v[36:39]\n\t" \
  "ds_write_b128 v129, v[16:19] offset:4096\n\t" \
  "s_nop 3\n\t" \
  "ds_write_b128 v129, v[20:23] offset:5120\n\t" \
  "ds_write_b128 v129, v[24:27] offset:6144\n\t" \
  "ds_write_b128 v129, v[28:31] offset:7168\n\t" \
  "ds_write_b128 v129, v[32:35] offset:8192\n\t" \
  "ds_write_b128 v129, v[36:39] offset:9216\n\t" \
  "s_waitcnt lgkmcnt(0)\n\t" \
  "s_barrier\n\t"
#define DOWN_CLOB "v0", "v1", "v2", "v3", "v4", "v5", "v6", "v7", "v8", "v9", "v10", "v11", "v12", "v13", "v14", "v15", "v16", "v17", "v18", "v19", "v20", "v21", "v22", "v23", "v24", "v25", "v26", "v27", "v28", "v29", "v30", "v31", "v32", "v33", "v34", "v35", "v36", "v37", "v38", "v39", "v40", "v41", "v42", "v43", "v44", "v45", "v46", "v47", "v48", "v49", "v50", "v51", "v52", "v53", "v54", "v55", "v56", "v57", "v58", "v59", "v60", "v61", "v62", "v63", "v64", "v65", "v66", "v67", "v68", "v69", "v70", "v71", "v72", "v73", "v74", "v75", "v76", "v77", "v78", "v79", "v80", "v81", "v82", "v83", "v84", "v85", "v86", "v87", "v88", "v89", "v90", "v91", "v92", "v93", "v94", "v95", "v96", "v97", "v98", "v99", "v100", "v101", "v102", "v103", "v104", "v105", "v106", "v107", "v108", "v109", "v110", "v111", "v112", "v113", "v114", "v115", "v116", "v117", "v118", "v119", "v124", "v125", "s44", "s45", "s46", "s47", "s48", "s49", "vcc", "scc", "memory"


constexpr u32 FL_E = 0, FL_P = 1024, FL_H = 16384, FL_O = 32768, FL_KV = 49152;
constexpr u32 WS_G = 8192;
constexpr u32 WS_KV = 65536;
constexpr u32 KVBLK = 3584;
constexpr u32 WS_W = 1u << 20;
constexpr u32 WS_A = 4u << 20;
constexpr u32 WS_END = 16u << 20;
constexpr u32 LDS_BYTES = 163840, SLOT = 46080, LQ = 138240, LK0 = 2 * SLOT;
constexpr float QSCALE = 0.22360679774997896f * 1.4426950408889634f;

struct Params { const char *x, *wq, *wk, *wv, *wo, *w1, *w3, *w2, *gw; char* out; char* ws; };

DI const char* psel(bool c, const char* a, const char* b) { asm("" : "+s"(a)); asm("" : "+s"(b)); return c ? a : b; }
DI u32 cvtpk(float lo, float hi) { f32x2 v = {lo, hi}; return __builtin_bit_cast(u32, __builtin_convertvector(v, bf16x2)); }
DI float blo(u32 u) { return __builtin_bit_cast(float, u << 16); }
DI float bhi(u32 u) { return __builtin_bit_cast(float, u & 0xffff0000u); }
DI bf16x8 asb(u32x4 v) { return __builtin_bit_cast(bf16x8, v); }
template <int IMM> DI void dma_sc1(u32 lds, u32 voff, const char* g) {
  asm volatile("s_mov_b32 m0, %0\n\ts_nop 0\n\tglobal_load_lds_dwordx4 %1, %2 offset:%3 sc1" ::"s"(__builtin_amdgcn_readfirstlane(lds - IMM)), "v"(voff), "s"(g), "i"(IMM) : "memory");
}
#define BDMA(v, r, so, imm) "buffer_load_dwordx4 " v ", " r ", " so " offen offset:" #imm " lds\n\t"
DI void req2(u32 lds, __amdgpu_buffer_rsrc_t r, u32 so, u32 v0, u32 v1) {
  asm volatile("s_mov_b32 m0, %0\n\ts_nop 0\n\t" BDMA("%3", "%1", "%2", 0) BDMA("%4", "%1", "%2", 1024) ::"s"(lds), "s"(r), "s"(so), "v"(v0), "v"(v1) : "memory"); }
DI void req2b(u32 lds, __amdgpu_buffer_rsrc_t r, u32 so, u32 v1, u32 v2) {
  asm volatile("s_mov_b32 m0, %0\n\ts_nop 0\n\t" BDMA("%3", "%1", "%2", 1024) BDMA("%4", "%1", "%2", 2048) ::"s"(lds), "s"(r), "s"(so), "v"(v1), "v"(v2) : "memory"); }
DI void req1(u32 lds, __amdgpu_buffer_rsrc_t r, u32 so, u32 v0) {
  asm volatile("s_mov_b32 m0, %0\n\ts_nop 0\n\t" BDMA("%3", "%1", "%2", 0) ::"s"(lds), "s"(r), "s"(so), "v"(v0) : "memory"); }
DI void req4(u32 lds, __amdgpu_buffer_rsrc_t r, u32 so, u32 v0, u32 v1, u32 v2, u32 v3) {
  asm volatile("s_mov_b32 m0, %0\n\ts_nop 0\n\t" BDMA("%3", "%1", "%2", 0) BDMA("%4", "%1", "%2", 1024) BDMA("%5", "%1", "%2", 2048) BDMA("%6", "%1", "%2", 3072)
               ::"s"(lds), "s"(r), "s"(so), "v"(v0), "v"(v1), "v"(v2), "v"(v3) : "memory"); }
#define WAITV(n) asm volatile("s_waitcnt vmcnt(" #n ")" ::: "memory")
#define WAITV_BAR(n) asm volatile("s_waitcnt vmcnt(" #n ") lgkmcnt(0)\n\ts_barrier" ::: "memory")
#define BAR() asm volatile("s_waitcnt lgkmcnt(0)\n\ts_barrier" ::: "memory")
DI u32x4 lds128(u32 a) { return *(const LAS u32x4*)(size_t)a; }
DI u32x2 ldstr(u32 a) { return __builtin_bit_cast(u32x2, __builtin_amdgcn_ds_read_tr16_b64_v4i16((LAS s16x4*)(size_t)a)); }
DI bf16x8 trfrag(u32 a0, u32 a1) { u32x2 p = ldstr(a0), q = ldstr(a1); return asb((u32x4){p[0], p[1], q[0], q[1]}); }
DI f32x16 mf32(bf16x8 a, bf16x8 b, f32x16 c) { return __builtin_amdgcn_mfma_f32_32x32x16_bf16(a, b, c, 0, 0, 0); }
DI void halves(float v, float& lo, float& hi) { const u32 u = __float_as_uint(v); auto r = __builtin_amdgcn_permlane32_swap(u, u, false, false); const u32 a = r[0], b = r[1]; lo = __uint_as_float(a); hi = __uint_as_float(b); }
DI float swap_hi(float v) { float a, b; halves(v, a, b); return b; }
DI float both_sum(float v) { float a, b; halves(v, a, b); return a + b; }
DI float both_max(float v) { float a, b; halves(v, a, b); return fmaxf(a, b); }
template <int CTRL, int RM> DI float dppmax(float v) { const int u = __float_as_int(v); return fmaxf(v, __int_as_float(__builtin_amdgcn_update_dpp(u, u, CTRL, RM, 0xf, false))); }
DI float wave_max(float v) {
  v = dppmax<0x111, 0xf>(v); v = dppmax<0x112, 0xf>(v); v = dppmax<0x114, 0xf>(v); v = dppmax<0x118, 0xf>(v); v = dppmax<0x142, 0xa>(v); v = dppmax<0x143, 0xc>(v);
  return __int_as_float(__builtin_amdgcn_readlane(__float_as_int(v), 63)); }
DI rsrc_t mkrsrc(const char* p, u32 bytes) { return __builtin_amdgcn_make_buffer_rsrc((void*)p, 0, bytes, 0x00020000); }
DI void st128(rsrc_t r, u32 off, u32x4 v) { __builtin_amdgcn_raw_buffer_store_b128(v, r, off, 0, 16); }
DI void st64(rsrc_t r, u32 off, u32x2 v) { __builtin_amdgcn_raw_buffer_store_b64(v, r, off, 0, 16); }
DI void st32(rsrc_t r, u32 off, u32 v) { __builtin_amdgcn_raw_buffer_store_b32(v, r, off, 0, 16); }
DI u32x4 ld128(rsrc_t r, u32 voff, u32 soff) { return __builtin_amdgcn_raw_buffer_load_b128(r, voff, soff, 16); }
DI u32x2 ld64(rsrc_t r, u32 voff, u32 soff) { return __builtin_amdgcn_raw_buffer_load_b64(r, voff, soff, 16); }
constexpr u32 SPIN_CAP = 1u << 22;
DI u64 ldflag(const Params& p, u32 off) { return __hip_atomic_load((const GAS u64*)(p.ws + off), RLX); }
DI void set_flag(const Params& p, u32 off, u64 v) { __hip_atomic_store((GAS u64*)(p.ws + off), v, RLX); }

DI u64 kv_store(const Params& p, const f32x4& a, const f32x4& b, int tb32, int isV, int w, int lane, u64 tag) {
  const int r = lane & 31, h = lane >> 5;
  const rsrc_t ws = mkrsrc(p.ws, WS_END);
  const u32 base = WS_KV + tb32 * KVBLK;
  float km = 0.f;
  if (isV) {
    u32x4 f = {cvtpk(a[0], a[1]), cvtpk(a[2], a[3]), cvtpk(b[0], b[1]), cvtpk(b[2], b[3])};
    if (r >= 20) { const u32 c1 = r == 20 ? 0x3F803F80u : 0u; f = (u32x4){c1, c1, c1, c1}; }
    st128(ws, base + 1024 + w * 1024 + lane * 16, f);
  } else {
    const u32 f0 = cvtpk(a[0] * QSCALE, a[1] * QSCALE), f1 = cvtpk(a[2] * QSCALE, a[3] * QSCALE);
    float n2 = blo(f0) * blo(f0) + bhi(f0) * bhi(f0) + blo(f1) * blo(f1) + bhi(f1) * bhi(f1);
    if (w == 0) {
      const u32 f2 = cvtpk(b[0] * QSCALE, b[1] * QSCALE), f3 = cvtpk(b[2] * QSCALE, b[3] * QSCALE);
      st128(ws, base + lane * 16, (u32x4){f0, f1, f2, f3});
      n2 = both_sum(n2 + blo(f2) * blo(f2) + bhi(f2) * bhi(f2) + blo(f3) * blo(f3) + bhi(f3) * bhi(f3));
    } else {
      const int R = (r & 19) | ((r & 4) << 1) | ((r & 8) >> 1);
      st64(ws, base + 3072 + lane * 8, h ? (u32x2){0x3F803F80u, 0x00003F80u} : (u32x2){f0, f1});
      if (h) { st32(ws, WS_G + (tb32 * 32 + R) * 4, __float_as_uint(__builtin_amdgcn_rcpf(1.f + __builtin_amdgcn_exp2f(a[0] * -1.4426950408889634f)))); n2 = 0.f; }
    }
    km = wave_max((n2 == n2) ? n2 : __builtin_inff());
  }
  return (tag << 24) | ((__float_as_uint(km) + 255u) >> 8);
}

constexpr u32 KX = 2 * SLOT, KW = KX + 40960, KG = KW + 25600;
DI void kv_issue(const Params& p, int tb32, int isV, int w, int lane) {
  const u32 LX = KX + w * 10240, LW = KW + w * 6400, LG = KG + w * 1280;
  { const rsrc_t r = mkrsrc(psel(isV, p.wv, p.wk), 25600); const u32 vo = lane * 16;
    req4(LW, r, w * 6400, vo, vo, vo, vo); req2(LW + 4096, r, w * 6400 + 4096, vo, vo); if (lane < 16) req1(LW + 6144, r, w * 6400 + 6144, vo); }
  { const rsrc_t r = mkrsrc(p.x + (size_t)tb32 * 32 * 1280 + w * 320 - 3072, 1u << 20); u32 vo[5];
#pragma unroll
    for (int i = 0; i < 5; ++i) { const u32 n = 64 * i + lane, rr = (n * 3277) >> 16, c = n - rr * 20 - (rr >> 2); vo[i] = rr * 1280 + (c >= 20 ? c + 20 : c) * 16 + (i < 4 ? 3072 - 1024 * i : 3072); }
    req4(LX, r, 0, vo[0], vo[1], vo[2], vo[3]); req1(LX + 4096, r, 0, vo[4]); req4(LX + 5120, r, 16 * 1280, vo[0], vo[1], vo[2], vo[3]); req1(LX + 9216, r, 16 * 1280, vo[4]); }
  if (!isV) {
#define GDMA "global_load_lds_ushort %1, %2 offset:"
    asm volatile("s_mov_b32 m0, %0\n\ts_nop 0\n\t" GDMA "0\n\ts_add_u32 m0, m0, 192\n\ts_nop 0\n\t" GDMA "64\n\ts_add_u32 m0, m0, 192\n\ts_nop 0\n\t" GDMA "128\n\ts_add_u32 m0, m0, 192\n\ts_nop 0\n\t"
                 GDMA "192\n\ts_add_u32 m0, m0, 192\n\ts_nop 0\n\t" GDMA "256" ::"s"(LG), "v"((lane >> 1) * 2), "s"(p.gw + w * 320) : "scc", "memory");
  }
}
DI f32x16 kv_partial(const Params& p, int isV, int w, int lane) {
  const int r = lane & 31, h = lane >> 5, blk = (lane >> 4) & 1, q = (lane & 15) >> 2, pp = lane & 3;
  const u32 LX = KX + w * 10240, LW = KW + w * 6400, LG = KG + w * 1280;
  const int R = isV ? r : ((r & 19) | ((r & 4) << 1) | ((r & 8) >> 1));
  const int c = 4 * blk + pp; const bool gl = (!isV) && c == 5;
  const u32 c0 = h + ((R >> 2) & 3), xa = LX + R * 320 + c0 * 16, x8 = xa + 256 - (c0 >= 4 ? 320 : 0), x9 = xa + 288 - (c0 >= 2 ? 320 : 0), st = gl ? 8 : 40, wa = (gl ? LG : LW + 8 * (c < 5 ? c : 4)) + (8 * h + q) * st;
  u32 wad[20];
#pragma unroll
  for (int ks = 0; ks < 10; ++ks) { wad[2 * ks] = wa + 16 * ks * st; wad[2 * ks + 1] = wa + (16 * ks + 4) * st; asm volatile("" : "+v"(wad[2 * ks]), "+v"(wad[2 * ks + 1])); }
  WAITV(0);
  f32x16 acc = {};
  bf16x8 xf[10], wf[10];
#pragma unroll
  for (int ks = 0; ks < 10; ++ks) { xf[ks] = asb(lds128(ks < 8 ? xa + ks * 32 : ks == 8 ? x8 : x9)); wf[ks] = trfrag(wad[2 * ks], wad[2 * ks + 1]); }
  if (isV) {
#pragma unroll
    for (int ks = 0; ks < 10; ++ks) acc = mf32(xf[ks], wf[ks], acc);
  } else {
#pragma unroll
    for (int ks = 0; ks < 10; ++ks) acc = mf32(wf[ks], xf[ks], acc);
  }
#pragma unroll
  for (int i = 0; i < 4; ++i) if (w ? (w > 1 || i < 2) : (i == 2 || (i == 3 && isV)))
    *(LAS f32x4*)(size_t)((w ? LW + i * 1024 : KG + (i - 2) * 1024) + lane * 16) = (f32x4){acc[4 * i], acc[4 * i + 1], acc[4 * i + 2], acc[4 * i + 3]};
  return acc;
}

DI u32 p_off(int ks, int i, int lane) {
  const u32 n = 64 * i + lane, rr = (n * 6554) >> 16, cc = n - rr * 10, g = rr >> 3, t = (rr >> 2) & 1, qq = rr & 3;
  u32 row;
  if (ks < 48) row = 32 * ks + 16 * (g >> 1) + 4 * (g & 1) + 8 * t + qq;
  else { const u32 P = 4 * (ks - 48) + g, hp = (P * 13108) >> 16, r5 = P - 5 * hp;
    row = 40 * hp + (r5 == 4 ? 16 + 20 * t : (r5 >> 1) * 20 + (r5 & 1) * 4 + 8 * t) + qq; }
  return row * 1280 + cc * 16;
}
DI void p_finish(const Params& p, int ks, int cg, u32 lds, int lane) {
  const rsrc_t ws = mkrsrc(p.ws, WS_END);
  const int g = lane >> 4, q = (lane & 15) >> 2, pp = lane & 3;
  const u32 a = lds + (8 * g + q) * 160;
#pragma unroll
  for (int c = 0; c < 5; ++c) {
    const u32 col = c < 4 ? 64 * (c >> 1) + 16 * pp + 8 * (c & 1) : 128 + 8 * pp;
    st128(ws, WS_W + ((cg * 68 + ks) * 5 + c) * 1024 + lane * 16, __builtin_bit_cast(u32x4, trfrag(a + col, a + col + 640)));
  }
}

DI void body(const Params& p) {
  const int lane = threadIdx.x & 63, w = __builtin_amdgcn_readfirstlane(threadIdx.x >> 6), f = blockIdx.x, xcd = f & 7, ii = f >> 3;
  const int r = lane & 31, h = lane >> 5, blk = (lane >> 4) & 1, q = (lane & 15) >> 2, pp = lane & 3;
  asm volatile("" ::"s"(p.x), "s"(p.w2), "s"(p.gw), "s"(p.ws));
  const int tb = (xcd >> 2) * 8 + (ii >> 2), cb = (xcd & 3) * 4 + (ii & 3);
  const int tg = xcd * 4 + (ii >> 3), cg = ii & 7;
  const bool doKV = f < 128; const int isV = ii & 1, kvb = (xcd >> 2) * 32 + (xcd & 3) * 8 + ((ii >> 1) & 7);
  const int tp = w & 1, cp = w >> 1, qh = tp, kq = cp;
  const int pf = f - 128, pu = w < 4 ? pf * 4 + w : 512 + pf; const bool doP = !doKV && (w < 4 || (w == 4 && pf < 32));
  u32x4 ad, qa1, qa2, gg; u32x2 k0a; u32 fad, pad;
  f32x16 acc[2][2]; u32x8 kvf;
  u64 ev, tag; u32 k0done; const int b0 = ii >> 1;
  {
    const int S0 = ((((ii >> 2) + ii) & 3) * 5 / 2 + (xcd >> 2)) % 10, S1 = S0 == 9 ? 0 : S0 + 1, S2 = S1 == 9 ? 0 : S1 + 1;
    f32x16 kacc;
    asm volatile("global_load_dwordx2 %0, %1, %2 sc1" : "={v[166:167]}"(ev) : "v"(FL_E), "s"(p.ws) : "memory");
    if (doKV && w < 4) kv_issue(p, kvb, isV, w, lane);
    const bool own = !doKV || w >= 4, four = doKV && w >= 4;
    const u32 lx = w * 2048, lw = 16384 + w * 3072, lq = 40960 + w * 1024;
    const rsrc_t rx = mkrsrc(p.x + (size_t)(128 * tb + 16 * w) * 1280 - 1024, 1u << 22);
    const u32 xv0 = (lane >> 3) * 1280 + (((lane & 7) ^ (lane >> 4)) << 4) + 1024, xv1 = (xv0 ^ 64) + 8 * 1280 - 1024;
    if (own) req2(lx, rx, S0 * 128, xv0, xv1);
    if (four) req2(lx - 8192, mkrsrc(p.x + (size_t)(128 * tb + 16 * w - 64) * 1280 - 1024, 1u << 22), S0 * 128, xv0, xv1);
    u32 wv[3], wu[3];
#pragma unroll
    for (int i = 0; i < 3; ++i) { const u32 n = 64 * i + lane, rr = (n * 5462) >> 16, cc = n - rr * 12; wu[i] = rr * (3072 - 192) + n * 16 + 2048 - 1024 * i; wv[i] = w >= 4 ? wu[i] + (cc >= 2 ? -32 : 160) : wu[i]; }
    const u32 wo = (16 * (w & 3)) * 3072 + cb * 192;
    const rsrc_t rw = mkrsrc(psel(w >= 4, p.w3, p.w1) + wo - 2048, 1u << 22), rq = mkrsrc(p.wq + cb * 80, 1u << 22);
    if (own) { req2(lw, rw, S0 * 64 * 3072, wv[0], wv[1]); req1(lw + 2048, rw, S0 * 64 * 3072 + 2048, wv[2]); }
    if (four) { const rsrc_t r1 = mkrsrc(p.w1 + wo - 2048, 1u << 22); req2(lw - 12288, r1, S0 * 64 * 3072, wu[0], wu[1]); req1(lw - 12288 + 2048, r1, S0 * 64 * 3072 + 2048, wu[2]); }
    const u32 qn = 64 * (w < 5 ? w : w - 4) + lane, qr = (qn * 13108) >> 16, qv = qr * 1200 + qn * 16;
    if (own && w < 5) req1(lq, rq, S0 * 64 * 1280, qv);
    if (four) req1(lq - 4096, rq, S0 * 64 * 1280, w == 4 ? ((lane * 13108) >> 16) * 1200 + lane * 16 : qv);
    if (own) { req2(SLOT + lx, rx, S1 * 128, xv0, xv1); req1(SLOT + lw, rw, S1 * 64 * 3072, wv[0]); }
    u32x4 xa, xb, wa, kvp = {};
#pragma unroll
    for (int i = 0; i < 4; ++i) { xa[i] = (64 * tp + r) * 128 + (((h ^ ((r >> 1) & 7)) << 4) ^ (i << 5)); xb[i] = xa[i] + 2 * SLOT; }
#pragma unroll
    for (int i = 0; i < 2; ++i) { const int T = 2 * cp + i, Ts = (pp >> 1) ? (T == 5 ? 0 : T + 1) : T, c = 4 * blk + pp;
      wa[i] = cp < 3 ? 16384 + (pp >> 1) * 12288 + (8 * h + q) * 192 + Ts * 32 + blk * 16 + (pp & 1) * 8 : 40960 + (8 * h + q) * 80 + i * 40 + 8 * (c < 5 ? c : 4);
      wa[2 + i] = wa[i] + 2 * SLOT; }
    asm volatile("" : "+v"(xa), "+v"(xb), "+v"(wa));
    { const u32 a16 = lane * 16;
      ad = (u32x4){a16, (u32)(h ? 32 : lane) * 8, (u32)(h * 32 + (r < 21 ? r : 21)) * 16, w * 9216 + a16};
#pragma unroll
      for (int t = 0; t < 4; ++t) { const u32 a = LQ + (4 * qh + ((kq + 1 + t) & 3)) * 1536; qa1[t] = a + a16; qa2[t] = a + 1024 + lane * 8; }
      k0a = (u32x2){LK0 + kq * 4096 + a16, LK0 + kq * 4096 + 3072 + lane * 8};
      gg = (u32x4){WS_G + (128 * tb + 64 * qh + 32 * (kq >> 1) + r) * 4, WS_G + (64 * tg + 32 * qh + (lane & 15)) * 4, 0u, 0u};
      const u32 l12 = lane < 12 ? lane : 0, lp = lane - 12, l3 = (l12 * 43) >> 7;
      fad = lane >= 12 && lane < 29 ? FL_P + (cg * 72 + (lp < 12 ? 12 * kq + lp : 36 + 5 * kq + lp)) * 8 : lane >= 29 && lane < 37 ? FL_H + (((tb * 2 + (lane & 1)) * 16 + cb) * 4 + ((lane - 29) >> 1)) * 8 : FL_H + ((tg * 16 + 4 * kq) * 4 + l3 + l12) * 8;
      pad = lane == 0 ? FL_H + (((tb * 2 + tp) * 16 + cb) * 4 + cp) * 8 : FL_P + ((pu & 7) * 72 + (pu >> 3)) * 8;
      asm volatile("" : "+v"(ad), "+v"(qa1), "+v"(qa2), "+v"(k0a), "+v"(gg), "+v"(fad), "+v"(pad)); }

    u32 lv[5] = {(u32)lane * 16, (u32)(lane & 31) * 16, 0u, 0u, 0u};
    if (doP) {
#pragma unroll
      for (int i = 0; i < 5; ++i) lv[i] = p_off(pu >> 3, i, lane) + (i < 4 ? 3072 - 1024 * i : 3072);
    }
    asm volatile("" : "+v"(lv[0]), "+v"(lv[1]), "+v"(lv[2]), "+v"(lv[3]), "+v"(lv[4]));
    if (!own) { kacc = kv_partial(p, isV, w, lane); req2(SLOT + lx, rx, S1 * 128, xv0, xv1); req1(SLOT + lw, rw, S1 * 64 * 3072, wv[0]); }
    WAITV_BAR(3);
    { u32 elo, ehi;
      asm volatile("v_readfirstlane_b32 %0, v166\n\tv_readfirstlane_b32 %1, v167\n\ts_nop 1" : "=s"(elo), "=s"(ehi) : "{v[166:167]}"(ev) : "memory");
      tag = (((u64)ehi << 32) | elo) + 1; }
    req2b(SLOT + lw, rw, S1 * 64 * 3072, wv[1], wv[2]);
    if (w < 5) req1(SLOT + lq, rq, S1 * 64 * 1280, qv);
    if (doKV && w < 2) {
      f32x4 u[3][2];
#pragma unroll
      for (int j = 0; j < 3; ++j)
#pragma unroll
        for (int i = 0; i < 2; ++i) u[j][i] = *(const LAS f32x4*)(size_t)((j ? KW + (j + 1) * 6400 + w * 2048 : w ? KG : KW + 6400) + i * 1024 + lane * 16);
      SB();
      const f32x4 a0 = w ? (f32x4){kacc[8], kacc[9], kacc[10], kacc[11]} : (f32x4){kacc[0], kacc[1], kacc[2], kacc[3]};
      const f32x4 a1 = w ? (f32x4){kacc[12], kacc[13], kacc[14], kacc[15]} : (f32x4){kacc[4], kacc[5], kacc[6], kacc[7]};
      const u64 word = kv_store(p, (a0 + u[0][0]) + (u[1][0] + u[2][0]), (a1 + u[0][1]) + (u[1][1] + u[2][1]), kvb, isV, w, lane, tag);
      kvp = (u32x4){FL_KV + (lane & 7) * 2048 + (kvb * 4 + isV * 2 + w) * 8, 0u, (u32)word, (u32)(word >> 32)};
    }
    const char* gk = cp < 3 ? psel(pu < 384, p.w2, p.wo) + (pu & 7) * 160 - 3072 : p.ws + WS_KV + (2 * tp * 16 + b0) * KVBLK;
    asm volatile(P1_ASM
      : "={v[0:15]}"(acc[0][0]), "={v[16:31]}"(acc[0][1]), "={v[32:47]}"(acc[1][0]), "={v[48:63]}"(acc[1][1]), "={v[136:143]}"(kvf), [k0] "=s"(k0done)
      : "{v[96:99]}"(xa), "{v[100:103]}"(xb), "{v[104:107]}"(wa), "{v108}"(xv0), "{v109}"(xv1), "{v110}"(wv[0]), "{v111}"(wv[1]), "{v112}"(wv[2]), "{v113}"(qv), "{v[114:117]}"(kvp), "{v126}"(FL_KV + xcd * 2048 + lane * 32), "{v127}"(lv[0]), "{v[128:131]}"((u32x4){lv[1], lv[2], lv[3], lv[4]}),
        [rx] "s"(rx), [rw] "s"(rw), [rq] "s"(rq), [st] "s"(S2), [lx] "s"(lx), [lw] "s"(lw), [lq] "s"(lq), [wv] "s"(w),
        [pub] "s"(doKV && w < 2 ? 1 : 99), [t40] "s"((tag << 24) >> 24), [lk] "s"(cp < 3 ? SLOT + w * 5120 : LK0 + tp * 8192), [gk] "s"(gk), [dp] "s"(doP ? 8 : 99), [wsb] "s"(p.ws)
      : P1_CLOB); }
  const rsrc_t ws = mkrsrc(p.ws, WS_END);
  if (cp < 3) {
#pragma unroll
    for (int mt = 0; mt < 2; ++mt) {
      const int token = 128 * tb + 64 * tp + 32 * mt + r;
#pragma unroll
      for (int i = 0; i < 2; ++i) {
        float hv[8];
#pragma unroll
        for (int e = 0; e < 8; ++e) { const float gt = acc[mt][i][(e >> 2) * 8 + (e & 3)], up = acc[mt][i][(e >> 2) * 8 + 4 + (e & 3)];
          hv[e] = gt * up * __builtin_amdgcn_rcpf(1.f + __builtin_amdgcn_exp2f(gt * -1.4426950408889634f)); }
        const int T = 6 * cb + 2 * cp + i;
        st128(ws, WS_A + ((token >> 6) * 68 + (T >> 1)) * 4096 + ((token >> 4) & 3) * 1024 + (2 * (T & 1) + h) * 256 + (token & 15) * 16,
              (u32x4){cvtpk(hv[0], hv[1]), cvtpk(hv[2], hv[3]), cvtpk(hv[4], hv[5]), cvtpk(hv[6], hv[7])});
      }
    }
    if (doP) { WAITV(4); p_finish(p, pu >> 3, pu & 7, SLOT + w * 5120, lane); }
  } else {
    u32 fa = kvf[0], fb = kvf[2];
    if (!k0done) {
      const u64 t40 = (tag << 24) >> 24; const u32 fo = FL_KV + xcd * 2048 + lane * 32;
      _Pragma("unroll 1") for (u32 s = 0; s < SPIN_CAP; ++s) { const u64 a = ldflag(p, fo), b = ldflag(p, fo + 8), c = ldflag(p, fo + 16), d = ldflag(p, fo + 24); fa = (u32)a; fb = (u32)b;
        if (__all((a >> 24) == t40 && (b >> 24) == t40 && (c >> 24) == t40 && (d >> 24) == t40)) break; __builtin_amdgcn_s_sleep(2); }
      __builtin_amdgcn_fence(__ATOMIC_ACQUIRE, "wavefront");
#pragma unroll
      for (int i = 0; i < 2; ++i) { const char* g = p.ws + WS_KV + ((2 * tp + i) * 16 + b0) * KVBLK; const u32 l = LK0 + (2 * tp + i) * 4096, vo = lane * 16;
        dma_sc1<0>(l, vo, g); dma_sc1<1024>(l + 1024, vo, g); dma_sc1<2048>(l + 2048, vo, g); dma_sc1<3072>(l + 3072, (lane & 31) * 16, g); }
    }
    const float km = __builtin_amdgcn_sqrtf(wave_max(__uint_as_float(fa << 8) + __uint_as_float(fb << 8))) * 1.02f;
    u32x4 q1[4]; u32x2 q2[4]; float nq[4];
#pragma unroll
    for (int t = 0; t < 4; ++t) { const f32x16& a = acc[t >> 1][t & 1];
      q1[t] = (u32x4){cvtpk(a[0], a[1]), cvtpk(a[2], a[3]), cvtpk(a[4], a[5]), cvtpk(a[6], a[7])};
      q2[t] = (u32x2){cvtpk(a[8], a[9]), cvtpk(a[10], a[11])};
      if (h) q2[t] = (u32x2){0u, 0u};
      float s = (a[8] * a[8] + a[9] * a[9]) + (a[10] * a[10] + a[11] * a[11]);
      s = h ? 0.f : s;
#pragma unroll
      for (int i = 0; i < 8; ++i) s = fmaf(a[i], a[i], s);
      nq[t] = __builtin_amdgcn_sqrtf(both_sum(s));
      *(LAS u32x4*)(size_t)(LQ + (4 * tp + t) * 1536 + lane * 16) = q1[t];
    }
    bool unsafe = false;
#pragma unroll
    for (int t = 0; t < 4; ++t) { nq[t] *= km; unsafe |= !(nq[t] <= 40.f); }
    if (__builtin_expect(__any(unsafe), 0)) {
      float mx[4] = {-__builtin_inff(), -__builtin_inff(), -__builtin_inff(), -__builtin_inff()};
#pragma unroll 1
      for (int jb = 0; jb < 64; ++jb) {
        const u32 base = WS_KV + jb * KVBLK;
        const u32x4 k1 = ld128(ws, lane * 16, base); const u32x2 k2 = ld64(ws, lane * 8, base + 3072);
#pragma unroll
        for (int t = 0; t < 4; ++t) {
          f32x16 s = mf32(asb(k1), asb(q1[t]), (f32x16){});
          s = __builtin_amdgcn_mfma_f32_32x32x8bf16_1k(__builtin_bit_cast(s16x4, k2), __builtin_bit_cast(s16x4, q2[t]), s, 0, 0, 0);
#pragma unroll
          for (int i = 0; i < 16; ++i) mx[t] = fmaxf(mx[t], s[i]);
        }
      }
#pragma unroll
      for (int t = 0; t < 4; ++t) { const float nm = -both_max(mx[t]); const u32 a = cvtpk(nm, 0.f); const float r1 = nm - blo(a); const u32 bq = cvtpk(r1, 0.f); const u32 cq = cvtpk(r1 - blo(bq), 0.f);
        if (h) q2[t] = (u32x2){(a & 0xffffu) | (bq << 16), cq & 0xffffu}; }
    } else {
#pragma unroll
      for (int t = 0; t < 4; ++t) if (h) q2[t][0] = cvtpk(-nq[t], 0.f);
    }
#pragma unroll
    for (int t = 0; t < 4; ++t) *(LAS u32x2*)(size_t)(LQ + (4 * tp + t) * 1536 + 1024 + lane * 8) = q2[t];
    WAITV(0);
  }
  f32x12 oa; u32x16 af[6];
  const char* gw0 = p.ws + WS_W + (cg * 68 + 12 * kq) * 5120 + qh * 3072;
  {
    const u32 pm = __builtin_amdgcn_readfirstlane(doP ? 3 : 1); const int b1 = (b0 + 1) & 15;
    asm volatile(ATT_ASM
      : "={v[36:47]}"(oa), "+{v[240:243]}"(gg), "+{v[52:55]}"(qa1), "+{v[56:59]}"(qa2), "+{v[60:61]}"(k0a), "={v[140:155]}"(af[0]), "={v[156:171]}"(af[1]), "={v[172:187]}"(af[2]), "={v[188:203]}"(af[3]), "={v[204:219]}"(af[4]), "={v[220:235]}"(af[5])
      : "{v137}"(fad), "{v[236:239]}"(ad), "{v244}"(pad),
        [rs] "s"(ws), [gb] "s"(WS_KV + (kq * 16 + b1) * KVBLK), [kb] "s"(WS_KV + kq * 16 * KVBLK), [ga] "s"(WS_A + (tg * 68 + 12 * kq) * 4096 + qh * 2048), [gw] "s"(WS_W + (cg * 68 + 12 * kq) * 5120 + qh * 3072), [lw] "s"(kq * 5120 + qh * 3072), [th] "s"(qh),
        [wsb] "s"(p.ws), [tag] "s"(tag), [pm] "s"(pm)
      : ATT_CLOB); }
  u32 ln; asm volatile("v_mbcnt_lo_u32_b32 %0, -1, 0\n\tv_mbcnt_hi_u32_b32 %0, -1, %0" : "=v"(ln));
  const u32 b16 = ln * 16, hh = ln >> 5; const int token = 128 * tb + 64 * qh + 32 * (kq >> 1) + (ln & 31), head = 2 * cb + (kq & 1);
  const float g = __uint_as_float(gg[0]); const u32 g0 = gg[2], g1 = gg[3];
  BAR();
  { f32x4 u[3][3];
#pragma unroll
    for (int i = 0; i < 3; ++i)
#pragma unroll
      for (int k = 0; k < 3; ++k) u[i][k] = *(const LAS f32x4*)(size_t)((((kq + 3 - i) & 3) * 2 + qh) * 9216 + i * 3072 + k * 1024 + b16);
    asm volatile("s_waitcnt lgkmcnt(0)\n\ts_barrier" : "+v"(u[0][0]), "+v"(u[0][1]), "+v"(u[0][2]), "+v"(u[1][0]), "+v"(u[1][1]), "+v"(u[1][2]), "+v"(u[2][0]), "+v"(u[2][1]), "+v"(u[2][2]) :: "memory");
#pragma unroll
    for (int i = 0; i < 3; ++i) { const char* gsrc = gw0 + (4 + i) * 5120; const u32 l = i * 20480 + kq * 5120 + qh * 3072;
      dma_sc1<0>(l, b16, gsrc); dma_sc1<1024>(l + 1024, b16, gsrc); if (qh == 0) dma_sc1<2048>(l + 2048, b16, gsrc);
      SB();
#pragma unroll
      for (int k = 0; k < 12; ++k) oa[k] += u[i][k >> 2][k & 3];
      asm volatile("" : "+v"(oa)); SB(); }
  }
  { const float sa = g * __builtin_amdgcn_rcpf(swap_hi(oa[8]));
    const int P0 = 5 * cb + 2 * (kq & 1) + hh, P4 = 5 * cb + 4;
    const u32 ob0 = WS_A + ((token >> 6) * 68 + 48) * 4096 + ((token >> 4) & 3) * 1024 + (token & 15) * 16;
    st128(ws, ob0 + (P0 >> 2) * 4096 + (P0 & 3) * 256, (u32x4){cvtpk(oa[0] * sa, oa[1] * sa), cvtpk(oa[2] * sa, oa[3] * sa), cvtpk(oa[4] * sa, oa[5] * sa), cvtpk(oa[6] * sa, oa[7] * sa)});
    st64(ws, hh ? 0x7ffffff0u : ob0 + (P4 >> 2) * 4096 + (P4 & 3) * 256 + (kq & 1) * 8, (u32x2){cvtpk(oa[8] * sa, oa[9] * sa), cvtpk(oa[10] * sa, oa[11] * sa)}); }
  { const u32x4 rb = {b16 + kq * 5120, b16 + kq * 5120 + 65536, b16 + kq * 5120 + 131072, FL_O + ((2 * tg + qh) * 32 + 8 * kq + (ln & 7)) * 8};
    const f32x2 og = {1.f - __uint_as_float(g0), 1.f - __uint_as_float(g1)};
    const char* ga = p.ws + WS_A + (tg * 68 + 48 + 5 * kq) * 4096 + qh * 2048;
    asm volatile(DOWN_ASM
      :
      : "{v[140:155]}"(af[0]), "{v[156:171]}"(af[1]), "{v[172:187]}"(af[2]), "{v[188:203]}"(af[3]), "{v[204:219]}"(af[4]), "{v[220:235]}"(af[5]),
        "{v[120:123]}"(rb), "{v[126:127]}"(og), "{v236}"(b16), "{v[128:129]}"((u32x2){FL_O + ((token >> 5) * 32 + head) * 8, w * 10240 + b16}),
        [gw] "s"(gw0 + 7 * 5120), [gwo] "s"(p.ws + WS_W + (cg * 68 + 48 + 5 * kq) * 5120 + qh * 3072), [lw] "s"(kq * 5120 + qh * 3072), [th] "s"(qh), [wsb] "s"(p.ws), [tag] "s"(tag),
        [ga0] "s"(ga), [ga1] "s"(ga + 4096), [ga2] "s"(ga + 8192), [ga3] "s"(ga + 12288), [ga4] "s"(ga + 16384)
      : DOWN_CLOB);
  }
  u64 e0 = 0, e1 = 0, e2 = 0, e3 = 0; const u32 eo = FL_O + ((ln >> 2) * 128 + (ln & 3) * 8) * 8;
  if (f == 255 && w == 7) { e0 = ldflag(p, eo); e1 = ldflag(p, eo + 16); e2 = ldflag(p, eo + 32); e3 = ldflag(p, eo + 48); }
  { const int mt = kq >> 1, cpair = kq & 1; const u32 a = qh * 10240 + b16;
    f32x4 u[3][4];
#pragma unroll
    for (int j = 0; j < 4; ++j) { u[0][j] = *(const LAS f32x4*)(size_t)(a + j * 20480 + (5 * mt + 2 * cpair) * 1024); u[1][j] = *(const LAS f32x4*)(size_t)(a + j * 20480 + (5 * mt + 2 * cpair + 1) * 1024);
      if (cpair == 0) u[2][j] = *(const LAS f32x4*)(size_t)(a + j * 20480 + (5 * mt + 4) * 1024); }
    SB();
    const f32x4 s0 = (u[0][0] + u[0][1]) + (u[0][2] + u[0][3]), s1 = (u[1][0] + u[1][1]) + (u[1][2] + u[1][3]);
    char* dst = p.out + (size_t)(64 * tg + 32 * qh + 16 * mt + (ln & 15)) * 1280 + cg * 160; const int gq = ln >> 4;
    const rsrc_t ro = mkrsrc(p.out, 2048 * 1280); const u32 oo = (u32)(dst - p.out);
    __builtin_amdgcn_raw_buffer_store_b128((u32x4){cvtpk(s0[0], s0[1]), cvtpk(s0[2], s0[3]), cvtpk(s1[0], s1[1]), cvtpk(s1[2], s1[3])}, ro, oo + cpair * 64 + gq * 16, 0, 0);
    if (cpair == 0) { const f32x4 s2 = (u[2][0] + u[2][1]) + (u[2][2] + u[2][3]); __builtin_amdgcn_raw_buffer_store_b64((u32x2){cvtpk(s2[0], s2[1]), cvtpk(s2[2], s2[3])}, ro, oo + 128 + gq * 8, 0, 0); } }
  if (f == 255 && w == 7) {
    _Pragma("unroll 1") for (u32 s = 0; s < SPIN_CAP; ++s) {
      if (__all(e0 == tag && e1 == tag && e2 == tag && e3 == tag)) break;
      __builtin_amdgcn_s_sleep(2); e0 = ldflag(p, eo); e1 = ldflag(p, eo + 16); e2 = ldflag(p, eo + 32); e3 = ldflag(p, eo + 48); }
    if (ln == 0) set_flag(p, FL_E, tag);
  }
}

__global__ __launch_bounds__(512, 2) void fused(Params p) { body(p); }

extern "C" void kernel_launch(void* const* d_in, const int* in_sizes, int n_in, void* d_out, int out_size, void* d_ws, size_t ws_size, hipStream_t stream) {
  Params p;
  p.x = (const char*)d_in[0]; p.wq = (const char*)d_in[1]; p.wk = (const char*)d_in[2]; p.wv = (const char*)d_in[3]; p.wo = (const char*)d_in[4];
  p.w1 = (const char*)d_in[6]; p.w3 = (const char*)d_in[7]; p.w2 = (const char*)d_in[8]; p.gw = (const char*)d_in[9];
  p.out = (char*)d_out; p.ws = (char*)d_ws;
  (void)hipFuncSetAttribute((const void*)fused, hipFuncAttributeMaxDynamicSharedMemorySize, LDS_BYTES);
  fused<<<256, 512, LDS_BYTES, stream>>>(p);
}
```
